```python
import torch

def _softmax(x, axis=-1):
    return torch.softmax(x, dim=axis)
def _gelu(x):
    return torch.nn.functional.gelu(x, approximate="tanh")
def _silu(x):
    return torch.nn.functional.silu(x)
def _sigmoid(x):
    return torch.sigmoid(x)
def _layernorm(x, axis=-1, eps=1e-5):
    m = x.mean(dim=axis, keepdim=True)
    v = x.var(dim=axis, keepdim=True, unbiased=False)
    return (x - m) / torch.sqrt(v + eps)
def _scatter_add(tgt, idx, src, axis=0):
    return tgt.clone().index_add_(axis, idx, src)
def _segment_sum(src, seg, n):
    out = torch.zeros((n,) + src.shape[1:], dtype=src.dtype)
    return out.index_add_(0, seg, src)
def _pad(x, pw):
    flat = tuple(v for pair in reversed(pw) for v in pair)
    return torch.nn.functional.pad(x, flat)

kernel_name = 'sequential_preLN__sliding_block_cached__glu_gated_bilinear__decode'

def setup_inputs():

    _w = (lambda a: (lambda t: t.to(torch.bfloat16) if t.is_floating_point() else t)(torch.as_tensor(a)))
    def _A_setup():
        B, Sq, D = 2, 1, 512
        Hq, Hkv, Sk, Dh = 32, 8, 32768, 128
        assert Hq % Hkv == 0, (Hq, Hkv)
        return {
            "x": _w((lambda *s: torch.randn(*s, dtype=torch.bfloat16) * 0.05)(B, Sq, D)),
            "k_cache": _w((lambda *s: torch.randn(*s, dtype=torch.bfloat16) * 1.6)(B, Hkv, Sk, Dh)),
            "v_cache": _w((lambda *s: torch.randn(*s, dtype=torch.bfloat16) * 0.8)(B, Hkv, Sk, Dh)),
            "wq": _w((lambda *s: torch.randn(*s, dtype=torch.bfloat16) * 1.6)(D, Hq * Dh) * (D ** -0.5)),
            "wo": _w((lambda *s: torch.randn(*s, dtype=torch.bfloat16) * 0.8)(Hq * Dh, D) * ((Hq * Dh) ** -0.5)),
        }
    _A_in = _A_setup()

    def _B_setup():
        B, S, D, F = 2, 1, 512, 1792
        return {
            "x": _w((lambda *s: torch.randn(*s, dtype=torch.bfloat16) * 0.05)(B, S, D)),
            "w_gate": _w((lambda *s: torch.randn(*s, dtype=torch.bfloat16) * 0.4)(D, F) * (D ** -0.5)),
            "w_up": _w((lambda *s: torch.randn(*s, dtype=torch.bfloat16) * 0.4)(D, F) * (D ** -0.5)),
            "w_down": _w((lambda *s: torch.randn(*s, dtype=torch.bfloat16) * 0.4)(F, D) * (F ** -0.5)),
        }
    _B_in = _B_setup()

    return {
        "A_x": _A_in["x"],
        "A_k_cache": _A_in["k_cache"],
        "A_v_cache": _A_in["v_cache"],
        "A_wq": _A_in["wq"],
        "A_wo": _A_in["wo"],
        "B_x": _B_in["x"],
        "B_w_gate": _B_in["w_gate"],
        "B_w_up": _B_in["w_up"],
        "B_w_down": _B_in["w_down"],
    }

def reference(A_x, A_k_cache, A_v_cache, A_wq, A_wo, B_x, B_w_gate, B_w_up, B_w_down):
    def _A_ref(x, k_cache, v_cache, wq, wo):
        B, Sq, D = x.shape
        Hq, Dh = 32, 128
        Hkv, Sk = k_cache.shape[1], k_cache.shape[2]
        W = min(4096, max(256, Sk // 4))
        rep = Hq // Hkv
        q = (lambda x, *d: x.permute(*d))((x @ wq).reshape(B, Sq, Hq, Dh), 0, 2, 1, 3)
        k = (lambda x, n, axis: torch.repeat_interleave(x, n, dim=axis))(k_cache[:, :, Sk - W:, :], rep, axis=1)
        v = (lambda x, n, axis: torch.repeat_interleave(x, n, dim=axis))(v_cache[:, :, Sk - W:, :], rep, axis=1)
        scores = torch.einsum("bhqd,bhkd->bhqk", q, k) * (Dh ** -0.5)
        p = _softmax(scores, axis=-1)
        o = torch.einsum("bhqk,bhkd->bhqd", p, v)
        return (lambda x, *d: x.permute(*d))(o, 0, 2, 1, 3).reshape(B, Sq, Hq * Dh) @ wo

    def _B_ref(x, w_gate, w_up, w_down):
        g = torch.einsum("bsd,df->bsf", x, w_gate)
        u = torch.einsum("bsd,df->bsf", x, w_up)
        h = g * u
        return torch.einsum("bsf,fd->bsd", h, w_down)

    x = A_x
    h = x + _A_ref(_layernorm(x, axis=-1), A_k_cache, A_v_cache, A_wq, A_wo)
    h2 = h + _B_ref(_layernorm(h, axis=-1), B_w_gate, B_w_up, B_w_down)
    return h2
```

```cpp
#include <hip/hip_runtime.h>
#include <hip/hip_bf16.h>
#include <stdint.h>

typedef unsigned short bf16_t;
__device__ __forceinline__ float bf2f(bf16_t b) { return __uint_as_float(((unsigned)b) << 16); }
__device__ __forceinline__ bf16_t f2bf(float f) { unsigned u = __float_as_uint(f); return (bf16_t)((u + 0x7fffu + ((u >> 16) & 1u)) >> 16); }

constexpr int B = 2, D = 512, HQ = 32, HKV = 8, SK = 32768, DH = 128, W = 4096, F = 1792;
constexpr int NSPLIT = 16, KS = W / NSPLIT;

__device__ __forceinline__ float block_sum(float v, float* red) {
  for (int o = 32; o > 0; o >>= 1) v += __shfl_xor(v, o);
  int w = threadIdx.x >> 6;
  __syncthreads();
  if ((threadIdx.x & 63) == 0) red[w] = v;
  __syncthreads();
  float s = 0.f;
  for (int i = 0; i < (int)(blockDim.x >> 6); ++i) s += red[i];
  return s;
}

__global__ void k_q(const bf16_t* x, const bf16_t* wq, float* q) {
  __shared__ float xn[B][D]; __shared__ float red[8];
  for (int b = 0; b < B; ++b) {
    float v0 = bf2f(x[b * D + threadIdx.x]), v1 = bf2f(x[b * D + 256 + threadIdx.x]);
    float m = block_sum(v0 + v1, red) / D;
    float var = block_sum((v0 - m) * (v0 - m) + (v1 - m) * (v1 - m), red) / D;
    float r = 1.f / sqrtf(var + 1e-5f);
    xn[b][threadIdx.x] = (v0 - m) * r; xn[b][256 + threadIdx.x] = (v1 - m) * r;
  }
  __syncthreads();
  int n = blockIdx.x * 256 + threadIdx.x;
  float a0 = 0.f, a1 = 0.f;
  for (int k = 0; k < D; ++k) { float w = bf2f(wq[(size_t)k * (HQ * DH) + n]); a0 += xn[0][k] * w; a1 += xn[1][k] * w; }
  q[n] = a0; q[HQ * DH + n] = a1;
}

__global__ void k_attn(const float* q, const bf16_t* kc, const bf16_t* vc, float* pm, float* pl, float* po) {
  __shared__ float qs[4][DH]; __shared__ float p[4][KS]; __shared__ float red[8];
  int s = blockIdx.x, kvh = blockIdx.y, b = blockIdx.z, t = threadIdx.x;
  for (int i = t; i < 4 * DH; i += 256) qs[i / DH][i % DH] = q[b * HQ * DH + kvh * 4 * DH + i];
  __syncthreads();
  const bf16_t* kp = kc + ((size_t)(b * HKV + kvh) * SK + (SK - W) + s * KS + t) * DH;
  float sc[4] = {0, 0, 0, 0};
  for (int d = 0; d < DH; ++d) { float kv = bf2f(kp[d]); for (int g = 0; g < 4; ++g) sc[g] += qs[g][d] * kv; }
  for (int g = 0; g < 4; ++g) {
    float v = sc[g] * 0.08838834764831845f;
    float m = v;
    for (int o = 32; o > 0; o >>= 1) m = fmaxf(m, __shfl_xor(m, o));
    __syncthreads();
    if ((t & 63) == 0) red[t >> 6] = m;
    __syncthreads();
    m = fmaxf(fmaxf(red[0], red[1]), fmaxf(red[2], red[3]));
    float e = __expf(v - m);
    float l = block_sum(e, red);
    p[g][t] = e;
    if (t == 0) { int row = (b * HQ + kvh * 4 + g) * NSPLIT + s; pm[row] = m; pl[row] = l; }
  }
  __syncthreads();
  const bf16_t* vp = vc + ((size_t)(b * HKV + kvh) * SK + (SK - W) + s * KS) * DH;
  int d = t & 127, gh = t >> 7;
  float o0 = 0.f, o1 = 0.f;
  for (int j = 0; j < KS; ++j) { float vv = bf2f(vp[(size_t)j * DH + d]); o0 += p[gh * 2][j] * vv; o1 += p[gh * 2 + 1][j] * vv; }
  po[((size_t)(b * HQ + kvh * 4 + gh * 2) * NSPLIT + s) * DH + d] = o0;
  po[((size_t)(b * HQ + kvh * 4 + gh * 2 + 1) * NSPLIT + s) * DH + d] = o1;
}

__global__ void k_comb(const float* pm, const float* pl, const float* po, float* o) {
  int row = blockIdx.x, d = threadIdx.x;
  float M = -INFINITY;
  for (int s = 0; s < NSPLIT; ++s) M = fmaxf(M, pm[row * NSPLIT + s]);
  float L = 0.f, acc = 0.f;
  for (int s = 0; s < NSPLIT; ++s) { float w = __expf(pm[row * NSPLIT + s] - M); L += w * pl[row * NSPLIT + s]; acc += w * po[((size_t)row * NSPLIT + s) * DH + d]; }
  o[row * DH + d] = acc / L;
}

__global__ void k_wo(const bf16_t* x, const float* o, const bf16_t* wo, float* h) {
  __shared__ float part[4][2][64];
  int c = threadIdx.x & 63, kq = threadIdx.x >> 6, n = blockIdx.x * 64 + c;
  float a0 = 0.f, a1 = 0.f;
  for (int k = kq * 1024; k < (kq + 1) * 1024; ++k) { float w = bf2f(wo[(size_t)k * D + n]); a0 += o[k] * w; a1 += o[HQ * DH + k] * w; }
  part[kq][0][c] = a0; part[kq][1][c] = a1;
  __syncthreads();
  if (kq < 2) { float s = part[0][kq][c] + part[1][kq][c] + part[2][kq][c] + part[3][kq][c]; h[kq * D + n] = bf2f(x[kq * D + n]) + s; }
}

__global__ void k_gu(const float* h, const bf16_t* wg, const bf16_t* wu, float* gu) {
  __shared__ float xn[B][D]; __shared__ float red[8];
  for (int b = 0; b < B; ++b) {
    float v0 = h[b * D + threadIdx.x], v1 = h[b * D + 256 + threadIdx.x];
    float m = block_sum(v0 + v1, red) / D;
    float var = block_sum((v0 - m) * (v0 - m) + (v1 - m) * (v1 - m), red) / D;
    float r = 1.f / sqrtf(var + 1e-5f);
    xn[b][threadIdx.x] = (v0 - m) * r; xn[b][256 + threadIdx.x] = (v1 - m) * r;
  }
  __syncthreads();
  int n = blockIdx.x * 256 + threadIdx.x;
  float g0 = 0.f, g1 = 0.f, u0 = 0.f, u1 = 0.f;
  for (int k = 0; k < D; ++k) { float a = bf2f(wg[(size_t)k * F + n]), c = bf2f(wu[(size_t)k * F + n]); g0 += xn[0][k] * a; g1 += xn[1][k] * a; u0 += xn[0][k] * c; u1 += xn[1][k] * c; }
  gu[n] = g0 * u0; gu[F + n] = g1 * u1;
}

__global__ void k_down(const float* h, const float* gu, const bf16_t* wd, bf16_t* out) {
  __shared__ float part[4][2][64];
  int c = threadIdx.x & 63, kq = threadIdx.x >> 6, n = blockIdx.x * 64 + c;
  float a0 = 0.f, a1 = 0.f;
  for (int k = kq * 448; k < (kq + 1) * 448; ++k) { float w = bf2f(wd[(size_t)k * D + n]); a0 += gu[k] * w; a1 += gu[F + k] * w; }
  part[kq][0][c] = a0; part[kq][1][c] = a1;
  __syncthreads();
  if (kq < 2) { float s = part[0][kq][c] + part[1][kq][c] + part[2][kq][c] + part[3][kq][c]; out[kq * D + n] = f2bf(h[kq * D + n] + s); }
}

extern "C" void kernel_launch(void* const* d_in, const int* in_sizes, int n_in, void* d_out, int out_size, void* d_ws, size_t ws_size, hipStream_t stream) {
  const bf16_t* x = (const bf16_t*)d_in[0]; const bf16_t* kc = (const bf16_t*)d_in[1]; const bf16_t* vc = (const bf16_t*)d_in[2];
  const bf16_t* wq = (const bf16_t*)d_in[3]; const bf16_t* wo = (const bf16_t*)d_in[4];
  const bf16_t* wg = (const bf16_t*)d_in[6]; const bf16_t* wu = (const bf16_t*)d_in[7]; const bf16_t* wd = (const bf16_t*)d_in[8];
  float* ws = (float*)d_ws;
  float* q = ws; float* pm = q + B * HQ * DH; float* pl = pm + B * HQ * NSPLIT; float* po = pl + B * HQ * NSPLIT; float* o = po + (size_t)B * HQ * NSPLIT * DH;
  float* h = o + B * HQ * DH; float* gu = h + B * D;
  k_q<<<16, 256, 0, stream>>>(x, wq, q);
  k_attn<<<dim3(NSPLIT, HKV, B), 256, 0, stream>>>(q, kc, vc, pm, pl, po);
  k_comb<<<B * HQ, 128, 0, stream>>>(pm, pl, po, o);
  k_wo<<<8, 256, 0, stream>>>(x, o, wo, h);
  k_gu<<<7, 256, 0, stream>>>(h, wg, wu, gu);
  k_down<<<8, 256, 0, stream>>>(h, gu, wd, (bf16_t*)d_out);
}
```

```cpp
#include <hip/hip_runtime.h>
#include <stdint.h>

typedef unsigned int u32;
typedef unsigned long long u64;
typedef u32 v4u __attribute__((ext_vector_type(4)));
typedef u32 v2u __attribute__((ext_vector_type(2)));
typedef float v4f __attribute__((ext_vector_type(4)));
typedef float v2f __attribute__((ext_vector_type(2)));
typedef short v8s __attribute__((ext_vector_type(8)));
typedef short v4s __attribute__((ext_vector_type(4)));
typedef __bf16 v2b __attribute__((ext_vector_type(2)));
typedef __amdgpu_buffer_rsrc_t rsrc_t;
#define LAS __attribute__((address_space(3)))
#define GAS __attribute__((address_space(1)))

#define QSLEEP 24
#define LATE_FFN 1

constexpr int SK = 32768, WIN = 4096;
constexpr u32 SENT = 0xFFFFFFFFu, OOB = 0x80000000u;
constexpr u32 SPIN_CAP = 1u << 18;
constexpr u32 WS_EPOCH = 0;
constexpr u32 WS_RDY = 4096;
constexpr u32 WS_QG = 8192;
constexpr u32 WS_OB = 524288;
constexpr u32 WS_LW = 786432;
constexpr u32 WS_HB = 802816;
constexpr u32 WS_HID = 819200;
constexpr u32 WS_BYTES = 1u << 20;
constexpr int L_STG = 0;
constexpr int L_ORED = 65536;
constexpr int L_Q = 81920;
constexpr int L_XN = 82944;
constexpr int L_MX = 84992;
constexpr int L_LS = 85120;
constexpr int L_CNT = 85248;
constexpr int L_H = 85264;
constexpr int L_CS = 85504;
constexpr int L_BYTES = 89600;

struct Args {
  const char *x, *kc, *vc, *wq, *wo, *wg, *wu, *wd;
  char *out, *ws;
};

#define STAMP(i) do {} while (0)
#define STAMP_OUT() do {} while (0)

__device__ __forceinline__ v4u ld_nt(const char* p) { return __builtin_nontemporal_load((const v4u*)p); }
__device__ __forceinline__ v4u ld_dev(rsrc_t r, u32 off) { return __builtin_amdgcn_raw_buffer_load_b128(r, off, 0, 16); }
__device__ __forceinline__ void st_dev(rsrc_t r, u32 off, v4u v) { __builtin_amdgcn_raw_buffer_store_b128(v, r, off, 0, 16); }
__device__ __forceinline__ void st_dev32(rsrc_t r, u32 off, u32 v) { __builtin_amdgcn_raw_buffer_store_b32(v, r, off, 0, 16); }
__device__ __forceinline__ void st_dev64(rsrc_t r, u32 off, v2u v) { __builtin_amdgcn_raw_buffer_store_b64(v, r, off, 0, 16); }
__device__ __forceinline__ float asf(u32 x) { return __builtin_bit_cast(float, x); }
__device__ __forceinline__ u32 asu(float x) { return __builtin_bit_cast(u32, x); }
__device__ __forceinline__ u32 pk2(float lo, float hi) { v2f f = {lo, hi}; return __builtin_bit_cast(u32, __builtin_convertvector(f, v2b)); }
__device__ __forceinline__ float bfr(float x) { return asf(pk2(x, x) << 16); }
__device__ __forceinline__ float bflo(u32 x) { return asf(x << 16); }
__device__ __forceinline__ float bfhi(u32 x) { return asf(x & 0xffff0000u); }
__device__ __forceinline__ float dot2(u32 x, u32 y, float c) { return __builtin_amdgcn_fdot2_f32_bf16(__builtin_bit_cast(v2b, x), __builtin_bit_cast(v2b, y), c, false); }
template <int CTRL> __device__ __forceinline__ float dppf(float x) { return asf((u32)__builtin_amdgcn_mov_dpp((int)asu(x), CTRL, 0xf, 0xf, true)); }
template <int CTRL> __device__ __forceinline__ u32 dppu(u32 x) { return (u32)__builtin_amdgcn_mov_dpp((int)x, CTRL, 0xf, 0xf, true); }
constexpr int XOR1 = 0xB1, XOR2 = 0x4E, HMIR = 0x141, ROR8 = 0x128;
__device__ __forceinline__ float fold32(float a, float b) { auto s = __builtin_amdgcn_permlane32_swap(asu(a), asu(b), false, false); return asf(s[0]) + asf(s[1]); }
__device__ __forceinline__ float fold16(float a, float b) { auto s = __builtin_amdgcn_permlane16_swap(asu(a), asu(b), false, false); return asf(s[0]) + asf(s[1]); }
__device__ __forceinline__ float max32(float a) { auto s = __builtin_amdgcn_permlane32_swap(asu(a), asu(a), false, false); return fmaxf(asf(s[0]), asf(s[1])); }
__device__ __forceinline__ float max16(float a) { auto s = __builtin_amdgcn_permlane16_swap(asu(a), asu(a), false, false); return fmaxf(asf(s[0]), asf(s[1])); }
__device__ __forceinline__ float wave_sum(float x) {
  x += dppf<XOR1>(x); x += dppf<XOR2>(x); x += dppf<HMIR>(x); x += dppf<ROR8>(x);
  x = fold16(x, x); return fold32(x, x);
}
__device__ __forceinline__ v4f mfma(v4u a, v4u b, v4f c) { return __builtin_amdgcn_mfma_f32_16x16x32_bf16(__builtin_bit_cast(v8s, a), __builtin_bit_cast(v8s, b), c, 0, 0, 0); }
__device__ __forceinline__ u32 max4(v4u v) { return max(max(v.x, v.y), max(v.z, v.w)); }
__device__ __forceinline__ v2u tr_read(LAS char* p) { return __builtin_bit_cast(v2u, __builtin_amdgcn_ds_read_tr16_b64_v4i16((LAS v4s*)p)); }
template <int RS> __device__ __forceinline__ v4u bfrag(LAS char* base, int lane) {
  LAS char* p = base + ((lane >> 4) * 8 + ((lane >> 2) & 3)) * RS + (lane & 3) * 8;
  v2u lo = tr_read(p), hi = tr_read(p + 4 * RS);
  return (v4u){lo.x, lo.y, hi.x, hi.y};
}
#define BARRIER() do { asm volatile("s_waitcnt lgkmcnt(0)" ::: "memory"); __builtin_amdgcn_s_barrier(); asm volatile("" ::: "memory"); } while (0)
__device__ __forceinline__ float ln_sd(float var) { return bfr(__builtin_amdgcn_sqrtf(bfr(bfr(var) + 1e-5f))); }

template <int N> __device__ __forceinline__ void gather(rsrc_t r, const u32 (&off)[N], v4u (&v)[N]) {
  u32 m, sp = 0;
  do {
    asm volatile("" ::: "memory");
#pragma unroll
    for (int k = 0; k < N; ++k) v[k] = ld_dev(r, off[k]);
    m = 0;
#pragma unroll
    for (int k = 0; k < N; ++k) m = max(m, max4(v[k]));
  } while (m == SENT && ++sp < SPIN_CAP);
}

__global__ void __launch_bounds__(576, 3) fused_decode(Args a) {
  __shared__ __attribute__((aligned(16))) char smem[L_BYTES];
  LAS char* lds = (LAS char*)smem;
  const int w = blockIdx.x, lane = threadIdx.x & 63, wave = __builtin_amdgcn_readfirstlane(threadIdx.x >> 6);
  const int kvh = w & 7, i = w >> 3, b = i >> 4, s = i & 15;
  const rsrc_t ws = __builtin_amdgcn_make_buffer_rsrc(a.ws, 0, WS_BYTES, 0x00020000);
  const int role = i < 16 ? 0 : i < 23 ? 1 : i < 27 ? 2 : 3;
  const int gidx = kvh * 7 + (i - 16), didx = kvh * 4 + (i - 23);
  STAMP(0);

  if (wave == 0) {
    const v2u ep = __builtin_amdgcn_raw_buffer_load_b64(ws, WS_EPOCH, 0, 16);
    const v4u x0 = *(const v4u*)(a.x + lane * 16), x1 = *(const v4u*)(a.x + 1024 + lane * 16);
    const int cgq = i & 7, kq = i >> 3, rl = lane >> 3, ch = lane & 7;
    v4u wq[16];
    const char* pq = a.wq + (size_t)(kq * 128 + rl * 16) * 8192 + kvh * 1024 + cgq * 128 + ch * 16;
#pragma unroll
    for (int j = 0; j < 16; ++j) wq[j] = ld_nt(pq + j * 8192);
    __builtin_amdgcn_sched_barrier(0);
    const v4u S4 = {SENT, SENT, SENT, SENT};
    st_dev(ws, WS_OB + (kvh * 32 + i) * 1024 + lane * 16, S4);
    st_dev32(ws, lane < 4 ? WS_LW + ((kvh * 2 + b) * 4 + lane) * 64 + s * 4 : OOB, SENT);
    st_dev(ws, (lane < 8 && role < 2) ? (role == 0 ? WS_HB + (kvh * 16 + i) * 128 : WS_HID + gidx * 128) + lane * 16 : OOB, S4);
    __builtin_amdgcn_sched_barrier(0);
    __builtin_amdgcn_s_barrier();
    __builtin_amdgcn_sched_barrier(0);
    __builtin_amdgcn_s_setprio(3);
    *(LAS u32*)(lds + L_CNT) = 0u;
    float f[2][8];
#pragma unroll
    for (int d = 0; d < 4; ++d) { f[0][2 * d] = bflo(x0[d]); f[0][2 * d + 1] = bfhi(x0[d]); f[1][2 * d] = bflo(x1[d]); f[1][2 * d + 1] = bfhi(x1[d]); }
#pragma unroll
    for (int r = 0; r < 2; ++r) {
      float sm = 0.f;
#pragma unroll
      for (int e = 0; e < 8; ++e) sm += f[r][e];
      const float mean = wave_sum(sm) * (1.f / 512.f);
      float sq = 0.f;
#pragma unroll
      for (int e = 0; e < 8; ++e) { const float t = f[r][e] - mean; sq = fmaf(t, t, sq); }
      const float inv = __builtin_amdgcn_rcpf(ln_sd(wave_sum(sq) * (1.f / 512.f))), mb = bfr(mean);
      v4u o;
#pragma unroll
      for (int d = 0; d < 4; ++d) o[d] = pk2(bfr(f[r][2 * d] - mb) * inv, bfr(f[r][2 * d + 1] - mb) * inv);
      *(LAS v4u*)(lds + L_XN + r * 1024 + lane * 16) = o;
    }
    v4u xp[2][2];
#pragma unroll
    for (int r = 0; r < 2; ++r) { LAS char* p = lds + L_XN + r * 1024 + kq * 256 + rl * 32; xp[r][0] = *(LAS v4u*)p; xp[r][1] = *(LAS v4u*)(p + 16); }
    STAMP(1);
    float acc[16];
#pragma unroll
    for (int e = 0; e < 16; ++e) acc[e] = 0.f;
#pragma unroll
    for (int u = 0; u < 8; ++u)
#pragma unroll
      for (int d = 0; d < 4; ++d) {
        const u32 lo = __builtin_amdgcn_perm(wq[2 * u + 1][d], wq[2 * u][d], 0x05040100u), hi = __builtin_amdgcn_perm(wq[2 * u + 1][d], wq[2 * u][d], 0x07060302u);
        acc[2 * d] = dot2(lo, xp[0][u >> 2][u & 3], acc[2 * d]); acc[2 * d + 1] = dot2(hi, xp[0][u >> 2][u & 3], acc[2 * d + 1]);
        acc[8 + 2 * d] = dot2(lo, xp[1][u >> 2][u & 3], acc[8 + 2 * d]); acc[9 + 2 * d] = dot2(hi, xp[1][u >> 2][u & 3], acc[9 + 2 * d]);
      }
#pragma unroll
    for (int e = 0; e < 8; ++e) acc[e] = fold32(acc[e], acc[8 + e]);
#pragma unroll
    for (int e = 0; e < 4; ++e) acc[e] = fold16(acc[e], acc[4 + e]);
    float q0, q1;
    { const bool up = lane & 8;
      q0 = (up ? acc[2] : acc[0]) + dppf<ROR8>(up ? acc[0] : acc[2]); q1 = (up ? acc[3] : acc[1]) + dppf<ROR8>(up ? acc[1] : acc[3]); }
    const u64 tag = (((u64)__builtin_amdgcn_readfirstlane(ep.y) << 32) | (u64)(u32)__builtin_amdgcn_readfirstlane(ep.x)) + 1ull;
    const u32 thi = (u32)(tag >> 8), tlo = (u32)tag << 24;
    asm volatile("s_waitcnt vmcnt(0)" ::: "memory");
    st_dev(ws, WS_QG + (((kvh * 2 + (lane >> 5)) * 4 + kq) * 512 + cgq * 64 + ch * 8 + ((lane >> 3) & 3) * 2   ) * 8,
           (v4u){((asu(q0) + 0x80u) >> 8) | tlo, thi, ((asu(q1) + 0x80u) >> 8) | tlo, thi});
    st_dev64(ws, lane == 0 ? WS_RDY + w * 8 : OOB, (v2u){(u32)tag, (u32)(tag >> 32)});
    STAMP(2);
    __builtin_amdgcn_s_sleep(QSLEEP);
    v4u g[16], rd[2];
    { u32 bad, sp = 0;
      do {
        asm volatile("" ::: "memory");
#pragma unroll
        for (int k = 0; k < 16; ++k) g[k] = ld_dev(ws, WS_QG + (kvh * 2 + b) * 16384 + k * 1024 + lane * 16);
        rd[0] = ld_dev(ws, WS_RDY + lane * 16); rd[1] = ld_dev(ws, WS_RDY + 1024 + lane * 16);
        u32 bh = 0, bl = 0;
#pragma unroll
        for (int k = 0; k < 16; ++k) { bh |= (g[k].y ^ thi) | (g[k].w ^ thi); bl |= (g[k].x ^ tlo) | (g[k].z ^ tlo); }
#pragma unroll
        for (int k = 0; k < 2; ++k) bh |= (rd[k].x ^ (u32)tag) | (rd[k].z ^ (u32)tag) | (rd[k].y ^ (u32)(tag >> 32)) | (rd[k].w ^ (u32)(tag >> 32));
        bad = bh | (bl >> 24);
      } while (__any(bad != 0u) && ++sp < SPIN_CAP);
    }
#pragma unroll
    for (int h = 0; h < 4; ++h) {
      const float c0 = (asf(g[h].x << 8) + asf(g[4 + h].x << 8)) + (asf(g[8 + h].x << 8) + asf(g[12 + h].x << 8));
      const float c1 = (asf(g[h].z << 8) + asf(g[4 + h].z << 8)) + (asf(g[8 + h].z << 8) + asf(g[12 + h].z << 8));
      *(LAS u32*)(lds + L_Q + h * 256 + lane * 4) = pk2(c0, c1);
    }
    STAMP(3);
    BARRIER();
    BARRIER();
    BARRIER();
    STAMP(4);
    if (LATE_FFN && role != 0) {
      u32 sp = 0;
#pragma unroll 1
      while (*(volatile LAS u32*)(lds + L_CNT) < 8u && ++sp < SPIN_CAP) __builtin_amdgcn_s_sleep(1);
    }
    if (role == 0) {
      const char* p = a.wo + (size_t)(kvh * 512 + (lane >> 2)) * 1024 + i * 64 + (lane & 3) * 16;
#pragma unroll 4
      for (int j = 0; j < 32; ++j) __builtin_amdgcn_global_load_lds((const GAS u32*)(p + j * 16384), (LAS u32*)(lds + L_STG + j * 1024), 16, 0, 2);
    } else if (role == 1) {
      const size_t o = (size_t)(lane >> 2) * 3584 + gidx * 64 + (lane & 3) * 16;
#pragma unroll 2
      for (int j = 0; j < 32; ++j) {
        __builtin_amdgcn_global_load_lds((const GAS u32*)(a.wg + o + j * 57344), (LAS u32*)(lds + L_STG + j * 1024), 16, 0, 2);
        __builtin_amdgcn_global_load_lds((const GAS u32*)(a.wu + o + j * 57344), (LAS u32*)(lds + L_STG + 32768 + j * 1024), 16, 0, 2);
      }
    } else if (role == 2) {
      const char* p = a.wd + (size_t)(lane >> 1) * 1024 + didx * 32 + (lane & 1) * 16;
#pragma unroll 4
      for (int j = 0; j < 56; ++j) __builtin_amdgcn_global_load_lds((const GAS u32*)(p + j * 32768), (LAS u32*)(lds + L_STG + j * 1024), 16, 0, 2);
    }
    STAMP(5);
    asm volatile("s_waitcnt vmcnt(0)" ::: "memory");
    STAMP(6);
    __builtin_amdgcn_s_barrier();
    if (w == 23 * 8) {
      __builtin_amdgcn_s_barrier();
      st_dev64(ws, lane == 0 ? WS_EPOCH : OOB, (v2u){(u32)tag, (u32)(tag >> 32)});
    }
    STAMP_OUT();
    return;
  }

  const int sw = wave - 1, g = lane >> 4, c = lane & 15;
  const int blk = (sw + i + kvh * 3 + (i >> 3)) & 7, rj = sw + i + kvh;
  const size_t kvbase = ((size_t)(b * 8 + kvh) * SK + (SK - WIN)) * 256 + (size_t)s * 65536 + blk * 8192 + lane * 16;
  v4u K[8], V[8];
  __builtin_amdgcn_s_barrier();
  __builtin_amdgcn_sched_barrier(0);
#pragma unroll
  for (int j = 0; j < 8; ++j) K[j] = ld_nt(a.kc + kvbase + ((j + rj) & 7) * 1024);
  __builtin_amdgcn_sched_barrier(0);
#pragma unroll
  for (int j = 0; j < 8; ++j) V[j] = ld_nt(a.vc + kvbase + ((j + rj) & 7) * 1024);
  __builtin_amdgcn_sched_barrier(0);
  STAMP(1);
  LAS char* stg = lds + L_STG + sw * 8192;
#pragma unroll
  for (int j = 0; j < 8; ++j) *(LAS v4u*)(stg + j * 1024 + g * 256 + ((c ^ (4 * g + (j & 3))) * 16)) = K[j];
  v4u kf[2][4];
#pragma unroll
  for (int t = 0; t < 2; ++t)
#pragma unroll
    for (int kk = 0; kk < 4; ++kk) kf[t][kk] = *(LAS v4u*)(stg + (4 * t + (c & 3)) * 1024 + (c >> 2) * 256 + (((4 * kk + g) ^ c) * 16));
  STAMP(2);
  BARRIER();
  STAMP(3);
  v4f sc[2] = {{0.f, 0.f, 0.f, 0.f}, {0.f, 0.f, 0.f, 0.f}};
#pragma unroll
  for (int kk = 0; kk < 4; ++kk) {
    const v4u qf = *(LAS v4u*)(lds + L_Q + (c >> 2) * 256 + (4 * kk + g) * 16);
    sc[0] = mfma(kf[0][kk], qf, sc[0]); sc[1] = mfma(kf[1][kk], qf, sc[1]);
  }
  float p[8];
#pragma unroll
  for (int j = 0; j < 8; ++j) p[j] = bfr(bfr(sc[j >> 2][j & 3]) * 0.08838834764831845f);
  float mx = fmaxf(fmaxf(fmaxf(p[0], p[1]), fmaxf(p[2], p[3])), fmaxf(fmaxf(p[4], p[5]), fmaxf(p[6], p[7])));
  mx = max32(max16(mx));
  if (lane < 16) *(LAS float*)(lds + L_MX + (c >> 2) * 32 + sw * 4) = mx;
  BARRIER();
  { const v4f m0 = *(LAS v4f*)(lds + L_MX + (c >> 2) * 32), m1 = *(LAS v4f*)(lds + L_MX + (c >> 2) * 32 + 16);
    mx = fmaxf(fmaxf(fmaxf(m0.x, m0.y), fmaxf(m0.z, m0.w)), fmaxf(fmaxf(m1.x, m1.y), fmaxf(m1.z, m1.w))); }
  const float mx2 = mx * 1.4426950408889634f;
  float ls = 0.f;
#pragma unroll
  for (int j = 0; j < 8; ++j) { p[j] = __builtin_amdgcn_exp2f((p[j] - mx) * 1.4426950408889634f); ls += p[j]; }
  ls = fold16(ls, ls); ls = fold32(ls, ls);
  if (lane < 16) *(LAS float*)(lds + L_LS + (c >> 2) * 32 + sw * 4) = ls;
  BARRIER();
  { const v4f l0 = *(LAS v4f*)(lds + L_LS + (c >> 2) * 32), l1 = *(LAS v4f*)(lds + L_LS + (c >> 2) * 32 + 16);
    ls = ((l0.x + l0.y) + (l0.z + l0.w)) + ((l1.x + l1.y) + (l1.z + l1.w)); }
  const float rl_ = __builtin_amdgcn_rcpf(ls);
  const v4u pf = {pk2(p[0] * rl_, p[1] * rl_), pk2(p[2] * rl_, p[3] * rl_), pk2(p[4] * rl_, p[5] * rl_), pk2(p[6] * rl_, p[7] * rl_)};
  const u32 lwbits = min(asu(mx2 + __builtin_amdgcn_logf(ls)), SENT - 1u);
  STAMP(4);
  v4f oa[8];
#pragma unroll
  for (int d = 0; d < 4; ++d) {
    v4u lo, hi;
#pragma unroll
    for (int m = 0; m < 4; ++m) { lo[m] = __builtin_amdgcn_perm(V[2 * m + 1][d], V[2 * m][d], 0x05040100u); hi[m] = __builtin_amdgcn_perm(V[2 * m + 1][d], V[2 * m][d], 0x07060302u); }
    oa[2 * d] = mfma(pf, lo, (v4f){0.f, 0.f, 0.f, 0.f}); oa[2 * d + 1] = mfma(pf, hi, (v4f){0.f, 0.f, 0.f, 0.f});
  }
  STAMP(5);
  *(LAS v4f*)(lds + L_ORED + sw * 2048 + lane * 16) = (v4f){oa[0].x, oa[1].x, oa[2].x, oa[3].x};
  *(LAS v4f*)(lds + L_ORED + sw * 2048 + 1024 + lane * 16) = (v4f){oa[4].x, oa[5].x, oa[6].x, oa[7].x};
  if (sw == 0) st_dev32(ws, (lane < 16 && (c & 3) == 0) ? WS_LW + ((kvh * 2 + b) * 4 + (c >> 2)) * 64 + s * 4 : OOB, lwbits);
  asm volatile("s_waitcnt lgkmcnt(0)" ::: "memory");
  if (lane == 0) __hip_atomic_fetch_add((LAS u32*)(lds + L_CNT), 1u, __ATOMIC_RELAXED, __HIP_MEMORY_SCOPE_WORKGROUP);
  if (sw < 4) {
    u32 sp = 0;
    while (*(volatile LAS u32*)(lds + L_CNT) < 8u && ++sp < SPIN_CAP) {}
    asm volatile("" ::: "memory");
    LAS char* rp = lds + L_ORED + ((lane >> 1) & 1) * 1024 + (sw * 16 + (lane >> 2)) * 16 + (lane & 1) * 8;
    v2f t[8];
#pragma unroll
    for (int u = 0; u < 8; ++u) t[u] = *(LAS v2f*)(rp + u * 2048);
    const v2f o2 = ((t[0] + t[1]) + (t[2] + t[3])) + ((t[4] + t[5]) + (t[6] + t[7]));
    const u32 ob = min(pk2(o2.x, o2.y), SENT - 1u);
    const v4u o4 = {ob, dppu<0x55>(ob), dppu<0xAA>(ob), dppu<0xFF>(ob)};
    st_dev(ws, (lane & 3) == 0 ? WS_OB + (kvh * 32 + i) * 1024 + sw * 256 + lane * 4 : OOB, o4);
  }
  STAMP(6);
  if (role == 3) { __builtin_amdgcn_s_barrier(); STAMP_OUT(); return; }

  if (role == 0) {
    v4u lw[2];
    { const u32 o0 = WS_LW + (kvh * 8 + (sw >> 1)) * 64 + g * 16; const u32 off[2] = {o0, o0 + 256}; gather<2>(ws, off, lw); }
    float wt[2][4];
#pragma unroll
    for (int r = 0; r < 2; ++r) {
      float m = fmaxf(fmaxf(asf(lw[r].x), asf(lw[r].y)), fmaxf(asf(lw[r].z), asf(lw[r].w)));
      m = max32(max16(m));
      float sm = 0.f;
#pragma unroll
      for (int e = 0; e < 4; ++e) { wt[r][e] = __builtin_amdgcn_exp2f(asf(lw[r][e]) - m); sm += wt[r][e]; }
      sm = fold16(sm, sm); sm = fold32(sm, sm);
      const float inv = __builtin_amdgcn_rcpf(sm);
#pragma unroll
      for (int e = 0; e < 4; ++e) wt[r][e] *= inv;
    }
    STAMP(7);
    __builtin_amdgcn_s_barrier();
    asm volatile("" ::: "memory");
    v4u bf[2][2];
#pragma unroll
    for (int ks = 0; ks < 2; ++ks)
#pragma unroll
      for (int nt = 0; nt < 2; ++nt) bf[ks][nt] = bfrag<64>(lds + L_STG + (sw * 64 + ks * 32) * 64 + nt * 32, lane);
    STAMP(8);
    v4u af[4];
    { const u32 o0 = WS_OB + (kvh * 32 + c) * 1024 + sw * 128 + g * 16; const u32 off[4] = {o0, o0 + 64, o0 + 16384, o0 + 16384 + 64}; gather<4>(ws, off, af); }
    STAMP(9);
    float val[2][2];
#pragma unroll
    for (int r = 0; r < 2; ++r)
#pragma unroll
      for (int nt = 0; nt < 2; ++nt) {
        v4f d = mfma(af[2 * r], bf[0][nt], (v4f){0.f, 0.f, 0.f, 0.f});
        d = mfma(af[2 * r + 1], bf[1][nt], d);
        val[r][nt] = fmaf(d.x, wt[r][0], d.y * wt[r][1]) + fmaf(d.z, wt[r][2], d.w * wt[r][3]);
      }
    const float y = fold16(fold32(val[0][0], val[1][0]), fold32(val[0][1], val[1][1]));
    *(LAS float*)(lds + L_ORED + sw * 256 + lane * 4) = y;
    BARRIER();
    if (sw == 0) {
      v2f t[8];
#pragma unroll
      for (int u = 0; u < 8; ++u) t[u] = *(LAS v2f*)(lds + L_ORED + u * 256 + (lane & 31) * 8);
      const v2f o2 = ((t[0] + t[1]) + (t[2] + t[3])) + ((t[4] + t[5]) + (t[6] + t[7]));
      st_dev32(ws, lane < 32 ? WS_HB + (kvh * 16 + i) * 128 + lane * 4 : OOB, min(pk2(o2.x, o2.y), SENT - 1u));
    }
    STAMP(10);
    STAMP_OUT();
    return;
  }

  if (role == 1) {
    v4u xa[2];
#pragma unroll
    for (int ks = 0; ks < 2; ++ks) { xa[ks] = *(const v4u*)(a.x + (c >> 3) * 1024 + sw * 128 + ks * 64 + g * 16); if (c & 7) xa[ks] = (v4u){0u, 0u, 0u, 0u}; }
    __builtin_amdgcn_s_barrier();
    asm volatile("" ::: "memory");
    STAMP(7);
    v4u bf[2][4];
#pragma unroll
    for (int ks = 0; ks < 2; ++ks)
#pragma unroll
      for (int q = 0; q < 4; ++q) bf[ks][q] = bfrag<64>(lds + L_STG + (q >> 1) * 32768 + (sw * 64 + ks * 32) * 64 + (q & 1) * 32, lane);
    const v4u ones = {0x3f803f80u, 0x3f803f80u, 0x3f803f80u, 0x3f803f80u};
    v4f ac[5];
    float cs[4];
#pragma unroll
    for (int q = 0; q < 4; ++q) {
      ac[q] = mfma(xa[1], bf[1][q], mfma(xa[0], bf[0][q], (v4f){0.f, 0.f, 0.f, 0.f}));
      cs[q] = mfma(ones, bf[1][q], mfma(ones, bf[0][q], (v4f){0.f, 0.f, 0.f, 0.f})).x;
    }
    ac[4] = mfma(xa[1], ones, mfma(xa[0], ones, (v4f){0.f, 0.f, 0.f, 0.f}));
    *(LAS v2f*)(lds + L_CS + sw * 512 + lane * 8) = (g & 1) ? (v2f){cs[1], cs[3]} : (v2f){cs[0], cs[2]};
    BARRIER();
    v2f csum;
    { v2f t[8];
#pragma unroll
      for (int u = 0; u < 8; ++u) t[u] = *(LAS v2f*)(lds + L_CS + u * 512 + lane * 8);
      csum = ((t[0] + t[1]) + (t[2] + t[3])) + ((t[4] + t[5]) + (t[6] + t[7])); }
    STAMP(8);
    v4u af[2];
    { const u32 o0 = WS_HB + ((c & 7) * 16 + sw * 2) * 128 + (c >> 3) * 64 + g * 16; const u32 off[2] = {o0, o0 + 128}; gather<2>(ws, off, af); }
    STAMP(9);
    float r5[5];
#pragma unroll
    for (int q = 0; q < 5; ++q) {
      const v4f d = mfma(af[1], q < 4 ? bf[1][q] : ones, mfma(af[0], q < 4 ? bf[0][q] : ones, ac[q]));
      r5[q] = (d.x + d.y) + (d.z + d.w);
    }
    *(LAS v4f*)(lds + L_ORED + sw * 1024 + lane * 16) = (v4f){fold16(r5[0], r5[1]), fold16(r5[2], r5[3]), fold16(r5[4], r5[4]), 0.f};
    BARRIER();
    if (sw == 0) {
      v4f t[8];
#pragma unroll
      for (int u = 0; u < 8; ++u) t[u] = *(LAS v4f*)(lds + L_ORED + u * 1024 + lane * 16);
      const v4f z = ((t[0] + t[1]) + (t[2] + t[3])) + ((t[4] + t[5]) + (t[6] + t[7]));
      const float mb = bfr(z.z * (1.f / 512.f));
      const float hid = fmaf(-mb, csum.x, z.x) * fmaf(-mb, csum.y, z.y);
      const u32 hb = min(pk2(hid, dppf<XOR1>(hid)), SENT - 1u);
      st_dev32(ws, (lane & 1) == 0 ? WS_HID + gidx * 128 + lane * 2 : OOB, hb);
    }
    STAMP(10);
    STAMP_OUT();
    return;
  }

  {
    v4u xr;
    if (sw < 2) xr = *(const v4u*)(a.x + sw * 1024 + lane * 16);
    __builtin_amdgcn_s_barrier();
    asm volatile("" ::: "memory");
    STAMP(7);
    v4u bf[7];
#pragma unroll
    for (int kb = 0; kb < 7; ++kb) bf[kb] = bfrag<32>(lds + L_STG + (sw * 7 + kb) * 1024, lane);
    if (sw < 2) {
      v4u hp[8];
      { u32 off[8];
#pragma unroll
        for (int u = 0; u < 8; ++u) off[u] = WS_HB + u * 2048 + (lane >> 2) * 128 + sw * 64 + (lane & 3) * 16;
        gather<8>(ws, off, hp); }
      float h[8], sm = 0.f;
#pragma unroll
      for (int d = 0; d < 4; ++d) {
        float lo = 0.f, hi = 0.f;
#pragma unroll
        for (int u = 0; u < 8; ++u) { lo += bflo(hp[u][d]); hi += bfhi(hp[u][d]); }
        h[2 * d] = bfr(bflo(xr[d]) + bfr(lo)); h[2 * d + 1] = bfr(bfhi(xr[d]) + bfr(hi));
        sm += h[2 * d] + h[2 * d + 1];
      }
      const float mean = wave_sum(sm) * (1.f / 512.f);
      float sq = 0.f;
#pragma unroll
      for (int e = 0; e < 8; ++e) { const float t = h[e] - mean; sq = fmaf(t, t, sq); }
      const float inv = __builtin_amdgcn_rcpf(ln_sd(wave_sum(sq) * (1.f / 512.f)));
      if ((lane >> 1) == didx) {
        *(LAS v4f*)(lds + L_H + sw * 64 + (lane & 1) * 32) = (v4f){h[0], h[1], h[2], h[3]};
        *(LAS v4f*)(lds + L_H + sw * 64 + (lane & 1) * 32 + 16) = (v4f){h[4], h[5], h[6], h[7]};
      }
      if (lane == 0) *(LAS float*)(lds + L_H + 128 + sw * 4) = inv * inv;
    }
    STAMP(8);
    v4u af[1];
    { const u32 off[1] = {(c & 7) < 7 ? WS_HID + (sw * 7 + (c & 7)) * 128 + (c >> 3) * 64 + g * 16 : OOB}; gather<1>(ws, off, af); }
    STAMP(9);
    v4f d[7];
#pragma unroll
    for (int kb = 0; kb < 7; ++kb) d[kb] = mfma(af[0], bf[kb], (v4f){0.f, 0.f, 0.f, 0.f});
    const bool odd = g & 1;
    float y = ((odd ? d[4].x : d[0].x) + (odd ? d[5].y : d[1].y)) + ((odd ? d[6].z : d[2].z) + (odd ? 0.f : d[3].w));
    y = fold16(y, y);
    *(LAS float*)(lds + L_ORED + sw * 256 + lane * 4) = y;
    BARRIER();
    if (sw == 0 && lane < 16) {
      const int r = lane >> 3, cp = lane & 7;
      v2f t[8];
#pragma unroll
      for (int u = 0; u < 8; ++u) t[u] = *(LAS v2f*)(lds + L_ORED + u * 256 + r * 128 + cp * 8);
      const v2f z = ((t[0] + t[1]) + (t[2] + t[3])) + ((t[4] + t[5]) + (t[6] + t[7]));
      const v2f hh = *(LAS v2f*)(lds + L_H + r * 64 + cp * 8);
      const float sc2 = *(LAS float*)(lds + L_H + 128 + r * 4);
      *(u32*)(a.out + r * 1024 + didx * 32 + cp * 4) = pk2(hh.x + bfr(z.x * sc2), hh.y + bfr(z.y * sc2));
    }
    STAMP(10);
    STAMP_OUT();
  }
}

extern "C" void kernel_launch(void* const* d_in, const int* in_sizes, int n_in, void* d_out, int out_size, void* d_ws, size_t ws_size,
                              hipStream_t stream) {
  Args a;
  a.x = (const char*)d_in[0]; a.kc = (const char*)d_in[1]; a.vc = (const char*)d_in[2]; a.wq = (const char*)d_in[3]; a.wo = (const char*)d_in[4];
  a.wg = (const char*)d_in[6]; a.wu = (const char*)d_in[7]; a.wd = (const char*)d_in[8];
  a.out = (char*)d_out; a.ws = (char*)d_ws;
  fused_decode<<<256, 576, 0, stream>>>(a);
}
```

```cpp
#define EXFIL 0
#include <hip/hip_runtime.h>
#include <stdint.h>

typedef unsigned int u32;
typedef unsigned long long u64;
typedef u32 v4u __attribute__((ext_vector_type(4)));
typedef u32 v2u __attribute__((ext_vector_type(2)));
typedef float v4f __attribute__((ext_vector_type(4)));
typedef float v2f __attribute__((ext_vector_type(2)));
typedef short v8s __attribute__((ext_vector_type(8)));
typedef short v4s __attribute__((ext_vector_type(4)));
typedef __bf16 v2b __attribute__((ext_vector_type(2)));
typedef __amdgpu_buffer_rsrc_t rsrc_t;
#define LAS __attribute__((address_space(3)))
#define GAS __attribute__((address_space(1)))

#define QSLEEP 24
#define LATE_FFN 1

constexpr int SK = 32768, WIN = 4096;
constexpr u32 SENT = 0xFFFFFFFFu, OOB = 0x80000000u;
constexpr u32 SPIN_CAP = 1u << 18;
constexpr u32 WS_EPOCH = 0;
constexpr u32 WS_RDY = 4096;
constexpr u32 WS_QG = 8192;
constexpr u32 WS_OB = 524288;
constexpr u32 WS_LW = 786432;
constexpr u32 WS_HB = 802816;
constexpr u32 WS_HID = 819200;
constexpr u32 WS_BYTES = 1u << 20;
constexpr int L_STG = 0;
constexpr int L_ORED = 65536;
constexpr int L_Q = 81920;
constexpr int L_XN = 82944;
constexpr int L_MX = 84992;
constexpr int L_LS = 85120;
constexpr int L_CNT = 85248;
constexpr int L_H = 85264;
constexpr int L_CS = 85504;
constexpr int L_BYTES = 89600 + 128;

struct Args {
  const char *x, *kc, *vc, *wq, *wo, *wg, *wu, *wd;
  char *out, *ws;
};

#define STAMP(i) do { __builtin_amdgcn_sched_barrier(0); ts[i] = __builtin_amdgcn_s_memrealtime(); __builtin_amdgcn_sched_barrier(0); if (wave == 8 && lane == 0) *(volatile LAS u64*)(lds + L_BYTES - 128 + (i) * 8) = ts[i]; } while (0)
#define STAMP_OUT() do { if (w == 184 && wave == 0) { asm volatile("s_waitcnt lgkmcnt(0)" ::: "memory"); u64 tk = EXFIL < 16 ? ts[EXFIL & 15] : *(volatile LAS u64*)(lds + L_BYTES - 128 + (EXFIL & 15) * 8); u64 until = ts[0] + 4000 + 10 * (tk - ts[0]); while (__builtin_amdgcn_s_memrealtime() < until) __builtin_amdgcn_s_sleep(2); } } while (0)

__device__ __forceinline__ v4u ld_nt(const char* p) { return __builtin_nontemporal_load((const v4u*)p); }
__device__ __forceinline__ v4u ld_dev(rsrc_t r, u32 off) { return __builtin_amdgcn_raw_buffer_load_b128(r, off, 0, 16); }
__device__ __forceinline__ void st_dev(rsrc_t r, u32 off, v4u v) { __builtin_amdgcn_raw_buffer_store_b128(v, r, off, 0, 16); }
__device__ __forceinline__ void st_dev32(rsrc_t r, u32 off, u32 v) { __builtin_amdgcn_raw_buffer_store_b32(v, r, off, 0, 16); }
__device__ __forceinline__ void st_dev64(rsrc_t r, u32 off, v2u v) { __builtin_amdgcn_raw_buffer_store_b64(v, r, off, 0, 16); }
__device__ __forceinline__ float asf(u32 x) { return __builtin_bit_cast(float, x); }
__device__ __forceinline__ u32 asu(float x) { return __builtin_bit_cast(u32, x); }
__device__ __forceinline__ u32 pk2(float lo, float hi) { v2f f = {lo, hi}; return __builtin_bit_cast(u32, __builtin_convertvector(f, v2b)); }
__device__ __forceinline__ float bfr(float x) { return asf(pk2(x, x) << 16); }
__device__ __forceinline__ float bflo(u32 x) { return asf(x << 16); }
__device__ __forceinline__ float bfhi(u32 x) { return asf(x & 0xffff0000u); }
__device__ __forceinline__ float dot2(u32 x, u32 y, float c) { return __builtin_amdgcn_fdot2_f32_bf16(__builtin_bit_cast(v2b, x), __builtin_bit_cast(v2b, y), c, false); }
template <int CTRL> __device__ __forceinline__ float dppf(float x) { return asf((u32)__builtin_amdgcn_mov_dpp((int)asu(x), CTRL, 0xf, 0xf, true)); }
template <int CTRL> __device__ __forceinline__ u32 dppu(u32 x) { return (u32)__builtin_amdgcn_mov_dpp((int)x, CTRL, 0xf, 0xf, true); }
constexpr int XOR1 = 0xB1, XOR2 = 0x4E, HMIR = 0x141, ROR8 = 0x128;
__device__ __forceinline__ float fold32(float a, float b) { auto s = __builtin_amdgcn_permlane32_swap(asu(a), asu(b), false, false); return asf(s[0]) + asf(s[1]); }
__device__ __forceinline__ float fold16(float a, float b) { auto s = __builtin_amdgcn_permlane16_swap(asu(a), asu(b), false, false); return asf(s[0]) + asf(s[1]); }
__device__ __forceinline__ float max32(float a) { auto s = __builtin_amdgcn_permlane32_swap(asu(a), asu(a), false, false); return fmaxf(asf(s[0]), asf(s[1])); }
__device__ __forceinline__ float max16(float a) { auto s = __builtin_amdgcn_permlane16_swap(asu(a), asu(a), false, false); return fmaxf(asf(s[0]), asf(s[1])); }
__device__ __forceinline__ float wave_sum(float x) {
  x += dppf<XOR1>(x); x += dppf<XOR2>(x); x += dppf<HMIR>(x); x += dppf<ROR8>(x);
  x = fold16(x, x); return fold32(x, x);
}
__device__ __forceinline__ v4f mfma(v4u a, v4u b, v4f c) { return __builtin_amdgcn_mfma_f32_16x16x32_bf16(__builtin_bit_cast(v8s, a), __builtin_bit_cast(v8s, b), c, 0, 0, 0); }
__device__ __forceinline__ u32 max4(v4u v) { return max(max(v.x, v.y), max(v.z, v.w)); }
__device__ __forceinline__ v2u tr_read(LAS char* p) { return __builtin_bit_cast(v2u, __builtin_amdgcn_ds_read_tr16_b64_v4i16((LAS v4s*)p)); }
template <int RS> __device__ __forceinline__ v4u bfrag(LAS char* base, int lane) {
  LAS char* p = base + ((lane >> 4) * 8 + ((lane >> 2) & 3)) * RS + (lane & 3) * 8;
  v2u lo = tr_read(p), hi = tr_read(p + 4 * RS);
  return (v4u){lo.x, lo.y, hi.x, hi.y};
}
#define BARRIER() do { asm volatile("s_waitcnt lgkmcnt(0)" ::: "memory"); __builtin_amdgcn_s_barrier(); asm volatile("" ::: "memory"); } while (0)
__device__ __forceinline__ float ln_sd(float var) { return bfr(__builtin_amdgcn_sqrtf(bfr(bfr(var) + 1e-5f))); }

template <int N> __device__ __forceinline__ void gather(rsrc_t r, const u32 (&off)[N], v4u (&v)[N]) {
  u32 m, sp = 0;
  do {
    asm volatile("" ::: "memory");
#pragma unroll
    for (int k = 0; k < N; ++k) v[k] = ld_dev(r, off[k]);
    m = 0;
#pragma unroll
    for (int k = 0; k < N; ++k) m = max(m, max4(v[k]));
  } while (m == SENT && ++sp < SPIN_CAP);
}

__global__ void __launch_bounds__(576, 3) fused_decode(Args a) {
  __shared__ __attribute__((aligned(16))) char smem[L_BYTES];
  LAS char* lds = (LAS char*)smem;
  const int w = blockIdx.x, lane = threadIdx.x & 63, wave = __builtin_amdgcn_readfirstlane(threadIdx.x >> 6);
  const int kvh = w & 7, i = w >> 3, b = i >> 4, s = i & 15;
  const rsrc_t ws = __builtin_amdgcn_make_buffer_rsrc(a.ws, 0, WS_BYTES, 0x00020000);
  const int role = i < 16 ? 0 : i < 23 ? 1 : i < 27 ? 2 : 3;
  const int gidx = kvh * 7 + (i - 16), didx = kvh * 4 + (i - 23);
  u64 ts[16] = {};
  STAMP(0);

  if (wave == 0) {
    const v2u ep = __builtin_amdgcn_raw_buffer_load_b64(ws, WS_EPOCH, 0, 16);
    const v4u x0 = *(const v4u*)(a.x + lane * 16), x1 = *(const v4u*)(a.x + 1024 + lane * 16);
    const int cgq = i & 7, kq = i >> 3, rl = lane >> 3, ch = lane & 7;
    v4u wq[16];
    const char* pq = a.wq + (size_t)(kq * 128 + rl * 16) * 8192 + kvh * 1024 + cgq * 128 + ch * 16;
#pragma unroll
    for (int j = 0; j < 16; ++j) wq[j] = ld_nt(pq + j * 8192);
    __builtin_amdgcn_sched_barrier(0);
    const v4u S4 = {SENT, SENT, SENT, SENT};
    st_dev(ws, WS_OB + (kvh * 32 + i) * 1024 + lane * 16, S4);
    st_dev32(ws, lane < 4 ? WS_LW + ((kvh * 2 + b) * 4 + lane) * 64 + s * 4 : OOB, SENT);
    st_dev(ws, (lane < 8 && role < 2) ? (role == 0 ? WS_HB + (kvh * 16 + i) * 128 : WS_HID + gidx * 128) + lane * 16 : OOB, S4);
    __builtin_amdgcn_sched_barrier(0);
    __builtin_amdgcn_s_barrier();
    __builtin_amdgcn_sched_barrier(0);
    __builtin_amdgcn_s_setprio(3);
    *(LAS u32*)(lds + L_CNT) = 0u;
    float f[2][8];
#pragma unroll
    for (int d = 0; d < 4; ++d) { f[0][2 * d] = bflo(x0[d]); f[0][2 * d + 1] = bfhi(x0[d]); f[1][2 * d] = bflo(x1[d]); f[1][2 * d + 1] = bfhi(x1[d]); }
#pragma unroll
    for (int r = 0; r < 2; ++r) {
      float sm = 0.f;
#pragma unroll
      for (int e = 0; e < 8; ++e) sm += f[r][e];
      const float mean = wave_sum(sm) * (1.f / 512.f);
      float sq = 0.f;
#pragma unroll
      for (int e = 0; e < 8; ++e) { const float t = f[r][e] - mean; sq = fmaf(t, t, sq); }
      const float inv = __builtin_amdgcn_rcpf(ln_sd(wave_sum(sq) * (1.f / 512.f))), mb = bfr(mean);
      v4u o;
#pragma unroll
      for (int d = 0; d < 4; ++d) o[d] = pk2(bfr(f[r][2 * d] - mb) * inv, bfr(f[r][2 * d + 1] - mb) * inv);
      *(LAS v4u*)(lds + L_XN + r * 1024 + lane * 16) = o;
    }
    v4u xp[2][2];
#pragma unroll
    for (int r = 0; r < 2; ++r) { LAS char* p = lds + L_XN + r * 1024 + kq * 256 + rl * 32; xp[r][0] = *(LAS v4u*)p; xp[r][1] = *(LAS v4u*)(p + 16); }
    STAMP(1);
    float acc[16];
#pragma unroll
    for (int e = 0; e < 16; ++e) acc[e] = 0.f;
#pragma unroll
    for (int u = 0; u < 8; ++u)
#pragma unroll
      for (int d = 0; d < 4; ++d) {
        const u32 lo = __builtin_amdgcn_perm(wq[2 * u + 1][d], wq[2 * u][d], 0x05040100u), hi = __builtin_amdgcn_perm(wq[2 * u + 1][d], wq[2 * u][d], 0x07060302u);
        acc[2 * d] = dot2(lo, xp[0][u >> 2][u & 3], acc[2 * d]); acc[2 * d + 1] = dot2(hi, xp[0][u >> 2][u & 3], acc[2 * d + 1]);
        acc[8 + 2 * d] = dot2(lo, xp[1][u >> 2][u & 3], acc[8 + 2 * d]); acc[9 + 2 * d] = dot2(hi, xp[1][u >> 2][u & 3], acc[9 + 2 * d]);
      }
#pragma unroll
    for (int e = 0; e < 8; ++e) acc[e] = fold32(acc[e], acc[8 + e]);
#pragma unroll
    for (int e = 0; e < 4; ++e) acc[e] = fold16(acc[e], acc[4 + e]);
    float q0, q1;
    { const bool up = lane & 8;
      q0 = (up ? acc[2] : acc[0]) + dppf<ROR8>(up ? acc[0] : acc[2]); q1 = (up ? acc[3] : acc[1]) + dppf<ROR8>(up ? acc[1] : acc[3]); }
    const u64 tag = (((u64)__builtin_amdgcn_readfirstlane(ep.y) << 32) | (u64)(u32)__builtin_amdgcn_readfirstlane(ep.x)) + 1ull;
    const u32 thi = (u32)(tag >> 8), tlo = (u32)tag << 24;
    asm volatile("s_waitcnt vmcnt(0)" ::: "memory");
    st_dev(ws, WS_QG + (((kvh * 2 + (lane >> 5)) * 4 + kq) * 512 + cgq * 64 + ch * 8 + ((lane >> 3) & 3) * 2   ) * 8,
           (v4u){((asu(q0) + 0x80u) >> 8) | tlo, thi, ((asu(q1) + 0x80u) >> 8) | tlo, thi});
    st_dev64(ws, lane == 0 ? WS_RDY + w * 8 : OOB, (v2u){(u32)tag, (u32)(tag >> 32)});
    STAMP(2);
    __builtin_amdgcn_s_sleep(QSLEEP);
    v4u g[16], rd[2];
    { u32 bad, sp = 0;
      do {
        asm volatile("" ::: "memory");
#pragma unroll
        for (int k = 0; k < 16; ++k) g[k] = ld_dev(ws, WS_QG + (kvh * 2 + b) * 16384 + k * 1024 + lane * 16);
        rd[0] = ld_dev(ws, WS_RDY + lane * 16); rd[1] = ld_dev(ws, WS_RDY + 1024 + lane * 16);
        u32 bh = 0, bl = 0;
#pragma unroll
        for (int k = 0; k < 16; ++k) { bh |= (g[k].y ^ thi) | (g[k].w ^ thi); bl |= (g[k].x ^ tlo) | (g[k].z ^ tlo); }
#pragma unroll
        for (int k = 0; k < 2; ++k) bh |= (rd[k].x ^ (u32)tag) | (rd[k].z ^ (u32)tag) | (rd[k].y ^ (u32)(tag >> 32)) | (rd[k].w ^ (u32)(tag >> 32));
        bad = bh | (bl >> 24);
      } while (__any(bad != 0u) && ++sp < SPIN_CAP);
    }
#pragma unroll
    for (int h = 0; h < 4; ++h) {
      const float c0 = (asf(g[h].x << 8) + asf(g[4 + h].x << 8)) + (asf(g[8 + h].x << 8) + asf(g[12 + h].x << 8));
      const float c1 = (asf(g[h].z << 8) + asf(g[4 + h].z << 8)) + (asf(g[8 + h].z << 8) + asf(g[12 + h].z << 8));
      *(LAS u32*)(lds + L_Q + h * 256 + lane * 4) = pk2(c0, c1);
    }
    STAMP(3);
    BARRIER();
    BARRIER();
    BARRIER();
    STAMP(4);
    if (LATE_FFN && role != 0) {
      u32 sp = 0;
#pragma unroll 1
      while (*(volatile LAS u32*)(lds + L_CNT) < 8u && ++sp < SPIN_CAP) __builtin_amdgcn_s_sleep(1);
    }
    if (role == 0) {
      const char* p = a.wo + (size_t)(kvh * 512 + (lane >> 2)) * 1024 + i * 64 + (lane & 3) * 16;
#pragma unroll 4
      for (int j = 0; j < 32; ++j) __builtin_amdgcn_global_load_lds((const GAS u32*)(p + j * 16384), (LAS u32*)(lds + L_STG + j * 1024), 16, 0, 2);
    } else if (role == 1) {
      const size_t o = (size_t)(lane >> 2) * 3584 + gidx * 64 + (lane & 3) * 16;
#pragma unroll 2
      for (int j = 0; j < 32; ++j) {
        __builtin_amdgcn_global_load_lds((const GAS u32*)(a.wg + o + j * 57344), (LAS u32*)(lds + L_STG + j * 1024), 16, 0, 2);
        __builtin_amdgcn_global_load_lds((const GAS u32*)(a.wu + o + j * 57344), (LAS u32*)(lds + L_STG + 32768 + j * 1024), 16, 0, 2);
      }
    } else if (role == 2) {
      const char* p = a.wd + (size_t)(lane >> 1) * 1024 + didx * 32 + (lane & 1) * 16;
#pragma unroll 4
      for (int j = 0; j < 56; ++j) __builtin_amdgcn_global_load_lds((const GAS u32*)(p + j * 32768), (LAS u32*)(lds + L_STG + j * 1024), 16, 0, 2);
    }
    STAMP(5);
    asm volatile("s_waitcnt vmcnt(0)" ::: "memory");
    STAMP(6);
    __builtin_amdgcn_s_barrier();
    if (w == 23 * 8) {
      __builtin_amdgcn_s_barrier();
      st_dev64(ws, lane == 0 ? WS_EPOCH : OOB, (v2u){(u32)tag, (u32)(tag >> 32)});
      STAMP(7);
    }
    STAMP_OUT();
    return;
  }

  const int sw = wave - 1, g = lane >> 4, c = lane & 15;
  const int blk = (sw + i + kvh * 3 + (i >> 3)) & 7, rj = sw + i + kvh;
  const size_t kvbase = ((size_t)(b * 8 + kvh) * SK + (SK - WIN)) * 256 + (size_t)s * 65536 + blk * 8192 + lane * 16;
  v4u K[8], V[8];
  __builtin_amdgcn_s_barrier();
  __builtin_amdgcn_sched_barrier(0);
#pragma unroll
  for (int j = 0; j < 8; ++j) K[j] = ld_nt(a.kc + kvbase + ((j + rj) & 7) * 1024);
  __builtin_amdgcn_sched_barrier(0);
#pragma unroll
  for (int j = 0; j < 8; ++j) V[j] = ld_nt(a.vc + kvbase + ((j + rj) & 7) * 1024);
  __builtin_amdgcn_sched_barrier(0);
  STAMP(1);
  LAS char* stg = lds + L_STG + sw * 8192;
#pragma unroll
  for (int j = 0; j < 8; ++j) *(LAS v4u*)(stg + j * 1024 + g * 256 + ((c ^ (4 * g + (j & 3))) * 16)) = K[j];
  v4u kf[2][4];
#pragma unroll
  for (int t = 0; t < 2; ++t)
#pragma unroll
    for (int kk = 0; kk < 4; ++kk) kf[t][kk] = *(LAS v4u*)(stg + (4 * t + (c & 3)) * 1024 + (c >> 2) * 256 + (((4 * kk + g) ^ c) * 16));
  STAMP(2);
  BARRIER();
  STAMP(3);
  v4f sc[2] = {{0.f, 0.f, 0.f, 0.f}, {0.f, 0.f, 0.f, 0.f}};
#pragma unroll
  for (int kk = 0; kk < 4; ++kk) {
    const v4u qf = *(LAS v4u*)(lds + L_Q + (c >> 2) * 256 + (4 * kk + g) * 16);
    sc[0] = mfma(kf[0][kk], qf, sc[0]); sc[1] = mfma(kf[1][kk], qf, sc[1]);
  }
  float p[8];
#pragma unroll
  for (int j = 0; j < 8; ++j) p[j] = bfr(bfr(sc[j >> 2][j & 3]) * 0.08838834764831845f);
  float mx = fmaxf(fmaxf(fmaxf(p[0], p[1]), fmaxf(p[2], p[3])), fmaxf(fmaxf(p[4], p[5]), fmaxf(p[6], p[7])));
  mx = max32(max16(mx));
  if (lane < 16) *(LAS float*)(lds + L_MX + (c >> 2) * 32 + sw * 4) = mx;
  BARRIER();
  { const v4f m0 = *(LAS v4f*)(lds + L_MX + (c >> 2) * 32), m1 = *(LAS v4f*)(lds + L_MX + (c >> 2) * 32 + 16);
    mx = fmaxf(fmaxf(fmaxf(m0.x, m0.y), fmaxf(m0.z, m0.w)), fmaxf(fmaxf(m1.x, m1.y), fmaxf(m1.z, m1.w))); }
  const float mx2 = mx * 1.4426950408889634f;
  float ls = 0.f;
#pragma unroll
  for (int j = 0; j < 8; ++j) { p[j] = __builtin_amdgcn_exp2f((p[j] - mx) * 1.4426950408889634f); ls += p[j]; }
  ls = fold16(ls, ls); ls = fold32(ls, ls);
  if (lane < 16) *(LAS float*)(lds + L_LS + (c >> 2) * 32 + sw * 4) = ls;
  BARRIER();
  { const v4f l0 = *(LAS v4f*)(lds + L_LS + (c >> 2) * 32), l1 = *(LAS v4f*)(lds + L_LS + (c >> 2) * 32 + 16);
    ls = ((l0.x + l0.y) + (l0.z + l0.w)) + ((l1.x + l1.y) + (l1.z + l1.w)); }
  const float rl_ = __builtin_amdgcn_rcpf(ls);
  const v4u pf = {pk2(p[0] * rl_, p[1] * rl_), pk2(p[2] * rl_, p[3] * rl_), pk2(p[4] * rl_, p[5] * rl_), pk2(p[6] * rl_, p[7] * rl_)};
  const u32 lwbits = min(asu(mx2 + __builtin_amdgcn_logf(ls)), SENT - 1u);
  STAMP(4);
  v4f oa[8];
#pragma unroll
  for (int d = 0; d < 4; ++d) {
    v4u lo, hi;
#pragma unroll
    for (int m = 0; m < 4; ++m) { lo[m] = __builtin_amdgcn_perm(V[2 * m + 1][d], V[2 * m][d], 0x05040100u); hi[m] = __builtin_amdgcn_perm(V[2 * m + 1][d], V[2 * m][d], 0x07060302u); }
    oa[2 * d] = mfma(pf, lo, (v4f){0.f, 0.f, 0.f, 0.f}); oa[2 * d + 1] = mfma(pf, hi, (v4f){0.f, 0.f, 0.f, 0.f});
  }
  STAMP(5);
  *(LAS v4f*)(lds + L_ORED + sw * 2048 + lane * 16) = (v4f){oa[0].x, oa[1].x, oa[2].x, oa[3].x};
  *(LAS v4f*)(lds + L_ORED + sw * 2048 + 1024 + lane * 16) = (v4f){oa[4].x, oa[5].x, oa[6].x, oa[7].x};
  if (sw == 0) st_dev32(ws, (lane < 16 && (c & 3) == 0) ? WS_LW + ((kvh * 2 + b) * 4 + (c >> 2)) * 64 + s * 4 : OOB, lwbits);
  asm volatile("s_waitcnt lgkmcnt(0)" ::: "memory");
  if (lane == 0) __hip_atomic_fetch_add((LAS u32*)(lds + L_CNT), 1u, __ATOMIC_RELAXED, __HIP_MEMORY_SCOPE_WORKGROUP);
  if (sw < 4) {
    u32 sp = 0;
    while (*(volatile LAS u32*)(lds + L_CNT) < 8u && ++sp < SPIN_CAP) {}
    asm volatile("" ::: "memory");
    LAS char* rp = lds + L_ORED + ((lane >> 1) & 1) * 1024 + (sw * 16 + (lane >> 2)) * 16 + (lane & 1) * 8;
    v2f t[8];
#pragma unroll
    for (int u = 0; u < 8; ++u) t[u] = *(LAS v2f*)(rp + u * 2048);
    const v2f o2 = ((t[0] + t[1]) + (t[2] + t[3])) + ((t[4] + t[5]) + (t[6] + t[7]));
    const u32 ob = min(pk2(o2.x, o2.y), SENT - 1u);
    const v4u o4 = {ob, dppu<0x55>(ob), dppu<0xAA>(ob), dppu<0xFF>(ob)};
    st_dev(ws, (lane & 3) == 0 ? WS_OB + (kvh * 32 + i) * 1024 + sw * 256 + lane * 4 : OOB, o4);
  }
  STAMP(6);
  if (role == 3) { __builtin_amdgcn_s_barrier(); STAMP_OUT(); return; }

  if (role == 0) {
    v4u lw[2];
    { const u32 o0 = WS_LW + (kvh * 8 + (sw >> 1)) * 64 + g * 16; const u32 off[2] = {o0, o0 + 256}; gather<2>(ws, off, lw); }
    float wt[2][4];
#pragma unroll
    for (int r = 0; r < 2; ++r) {
      float m = fmaxf(fmaxf(asf(lw[r].x), asf(lw[r].y)), fmaxf(asf(lw[r].z), asf(lw[r].w)));
      m = max32(max16(m));
      float sm = 0.f;
#pragma unroll
      for (int e = 0; e < 4; ++e) { wt[r][e] = __builtin_amdgcn_exp2f(asf(lw[r][e]) - m); sm += wt[r][e]; }
      sm = fold16(sm, sm); sm = fold32(sm, sm);
      const float inv = __builtin_amdgcn_rcpf(sm);
#pragma unroll
      for (int e = 0; e < 4; ++e) wt[r][e] *= inv;
    }
    STAMP(7);
    __builtin_amdgcn_s_barrier();
    asm volatile("" ::: "memory");
    v4u bf[2][2];
#pragma unroll
    for (int ks = 0; ks < 2; ++ks)
#pragma unroll
      for (int nt = 0; nt < 2; ++nt) bf[ks][nt] = bfrag<64>(lds + L_STG + (sw * 64 + ks * 32) * 64 + nt * 32, lane);
    STAMP(8);
    v4u af[4];
    { const u32 o0 = WS_OB + (kvh * 32 + c) * 1024 + sw * 128 + g * 16; const u32 off[4] = {o0, o0 + 64, o0 + 16384, o0 + 16384 + 64}; gather<4>(ws, off, af); }
    STAMP(9);
    float val[2][2];
#pragma unroll
    for (int r = 0; r < 2; ++r)
#pragma unroll
      for (int nt = 0; nt < 2; ++nt) {
        v4f d = mfma(af[2 * r], bf[0][nt], (v4f){0.f, 0.f, 0.f, 0.f});
        d = mfma(af[2 * r + 1], bf[1][nt], d);
        val[r][nt] = fmaf(d.x, wt[r][0], d.y * wt[r][1]) + fmaf(d.z, wt[r][2], d.w * wt[r][3]);
      }
    const float y = fold16(fold32(val[0][0], val[1][0]), fold32(val[0][1], val[1][1]));
    *(LAS float*)(lds + L_ORED + sw * 256 + lane * 4) = y;
    BARRIER();
    if (sw == 0) {
      v2f t[8];
#pragma unroll
      for (int u = 0; u < 8; ++u) t[u] = *(LAS v2f*)(lds + L_ORED + u * 256 + (lane & 31) * 8);
      const v2f o2 = ((t[0] + t[1]) + (t[2] + t[3])) + ((t[4] + t[5]) + (t[6] + t[7]));
      st_dev32(ws, lane < 32 ? WS_HB + (kvh * 16 + i) * 128 + lane * 4 : OOB, min(pk2(o2.x, o2.y), SENT - 1u));
    }
    STAMP(10);
    STAMP_OUT();
    return;
  }

  if (role == 1) {
    v4u xa[2];
#pragma unroll
    for (int ks = 0; ks < 2; ++ks) { xa[ks] = *(const v4u*)(a.x + (c >> 3) * 1024 + sw * 128 + ks * 64 + g * 16); if (c & 7) xa[ks] = (v4u){0u, 0u, 0u, 0u}; }
    __builtin_amdgcn_s_barrier();
    asm volatile("" ::: "memory");
    STAMP(7);
    v4u bf[2][4];
#pragma unroll
    for (int ks = 0; ks < 2; ++ks)
#pragma unroll
      for (int q = 0; q < 4; ++q) bf[ks][q] = bfrag<64>(lds + L_STG + (q >> 1) * 32768 + (sw * 64 + ks * 32) * 64 + (q & 1) * 32, lane);
    const v4u ones = {0x3f803f80u, 0x3f803f80u, 0x3f803f80u, 0x3f803f80u};
    v4f ac[5];
    float cs[4];
#pragma unroll
    for (int q = 0; q < 4; ++q) {
      ac[q] = mfma(xa[1], bf[1][q], mfma(xa[0], bf[0][q], (v4f){0.f, 0.f, 0.f, 0.f}));
      cs[q] = mfma(ones, bf[1][q], mfma(ones, bf[0][q], (v4f){0.f, 0.f, 0.f, 0.f})).x;
    }
    ac[4] = mfma(xa[1], ones, mfma(xa[0], ones, (v4f){0.f, 0.f, 0.f, 0.f}));
    *(LAS v2f*)(lds + L_CS + sw * 512 + lane * 8) = (g & 1) ? (v2f){cs[1], cs[3]} : (v2f){cs[0], cs[2]};
    BARRIER();
    v2f csum;
    { v2f t[8];
#pragma unroll
      for (int u = 0; u < 8; ++u) t[u] = *(LAS v2f*)(lds + L_CS + u * 512 + lane * 8);
      csum = ((t[0] + t[1]) + (t[2] + t[3])) + ((t[4] + t[5]) + (t[6] + t[7])); }
    STAMP(8);
    v4u af[2];
    { const u32 o0 = WS_HB + ((c & 7) * 16 + sw * 2) * 128 + (c >> 3) * 64 + g * 16; const u32 off[2] = {o0, o0 + 128}; gather<2>(ws, off, af); }
    STAMP(9);
    float r5[5];
#pragma unroll
    for (int q = 0; q < 5; ++q) {
      const v4f d = mfma(af[1], q < 4 ? bf[1][q] : ones, mfma(af[0], q < 4 ? bf[0][q] : ones, ac[q]));
      r5[q] = (d.x + d.y) + (d.z + d.w);
    }
    *(LAS v4f*)(lds + L_ORED + sw * 1024 + lane * 16) = (v4f){fold16(r5[0], r5[1]), fold16(r5[2], r5[3]), fold16(r5[4], r5[4]), 0.f};
    BARRIER();
    if (sw == 0) {
      v4f t[8];
#pragma unroll
      for (int u = 0; u < 8; ++u) t[u] = *(LAS v4f*)(lds + L_ORED + u * 1024 + lane * 16);
      const v4f z = ((t[0] + t[1]) + (t[2] + t[3])) + ((t[4] + t[5]) + (t[6] + t[7]));
      const float mb = bfr(z.z * (1.f / 512.f));
      const float hid = fmaf(-mb, csum.x, z.x) * fmaf(-mb, csum.y, z.y);
      const u32 hb = min(pk2(hid, dppf<XOR1>(hid)), SENT - 1u);
      st_dev32(ws, (lane & 1) == 0 ? WS_HID + gidx * 128 + lane * 2 : OOB, hb);
    }
    STAMP(10);
    STAMP_OUT();
    return;
  }

  {
    v4u xr;
    if (sw < 2) xr = *(const v4u*)(a.x + sw * 1024 + lane * 16);
    __builtin_amdgcn_s_barrier();
    asm volatile("" ::: "memory");
    STAMP(7);
    v4u bf[7];
#pragma unroll
    for (int kb = 0; kb < 7; ++kb) bf[kb] = bfrag<32>(lds + L_STG + (sw * 7 + kb) * 1024, lane);
    if (sw < 2) {
      v4u hp[8];
      { u32 off[8];
#pragma unroll
        for (int u = 0; u < 8; ++u) off[u] = WS_HB + u * 2048 + (lane >> 2) * 128 + sw * 64 + (lane & 3) * 16;
        gather<8>(ws, off, hp); }
      float h[8], sm = 0.f;
#pragma unroll
      for (int d = 0; d < 4; ++d) {
        float lo = 0.f, hi = 0.f;
#pragma unroll
        for (int u = 0; u < 8; ++u) { lo += bflo(hp[u][d]); hi += bfhi(hp[u][d]); }
        h[2 * d] = bfr(bflo(xr[d]) + bfr(lo)); h[2 * d + 1] = bfr(bfhi(xr[d]) + bfr(hi));
        sm += h[2 * d] + h[2 * d + 1];
      }
      const float mean = wave_sum(sm) * (1.f / 512.f);
      float sq = 0.f;
#pragma unroll
      for (int e = 0; e < 8; ++e) { const float t = h[e] - mean; sq = fmaf(t, t, sq); }
      const float inv = __builtin_amdgcn_rcpf(ln_sd(wave_sum(sq) * (1.f / 512.f)));
      if ((lane >> 1) == didx) {
        *(LAS v4f*)(lds + L_H + sw * 64 + (lane & 1) * 32) = (v4f){h[0], h[1], h[2], h[3]};
        *(LAS v4f*)(lds + L_H + sw * 64 + (lane & 1) * 32 + 16) = (v4f){h[4], h[5], h[6], h[7]};
      }
      if (lane == 0) *(LAS float*)(lds + L_H + 128 + sw * 4) = inv * inv;
    }
    STAMP(8);
    v4u af[1];
    { const u32 off[1] = {(c & 7) < 7 ? WS_HID + (sw * 7 + (c & 7)) * 128 + (c >> 3) * 64 + g * 16 : OOB}; gather<1>(ws, off, af); }
    STAMP(9);
    v4f d[7];
#pragma unroll
    for (int kb = 0; kb < 7; ++kb) d[kb] = mfma(af[0], bf[kb], (v4f){0.f, 0.f, 0.f, 0.f});
    const bool odd = g & 1;
    float y = ((odd ? d[4].x : d[0].x) + (odd ? d[5].y : d[1].y)) + ((odd ? d[6].z : d[2].z) + (odd ? 0.f : d[3].w));
    y = fold16(y, y);
    *(LAS float*)(lds + L_ORED + sw * 256 + lane * 4) = y;
    BARRIER();
    if (sw == 0 && lane < 16) {
      const int r = lane >> 3, cp = lane & 7;
      v2f t[8];
#pragma unroll
      for (int u = 0; u < 8; ++u) t[u] = *(LAS v2f*)(lds + L_ORED + u * 256 + r * 128 + cp * 8);
      const v2f z = ((t[0] + t[1]) + (t[2] + t[3])) + ((t[4] + t[5]) + (t[6] + t[7]));
      const v2f hh = *(LAS v2f*)(lds + L_H + r * 64 + cp * 8);
      const float sc2 = *(LAS float*)(lds + L_H + 128 + r * 4);
      *(u32*)(a.out + r * 1024 + didx * 32 + cp * 4) = pk2(hh.x + bfr(z.x * sc2), hh.y + bfr(z.y * sc2));
    }
    STAMP(10);
    STAMP_OUT();
  }
}

extern "C" void kernel_launch(void* const* d_in, const int* in_sizes, int n_in, void* d_out, int out_size, void* d_ws, size_t ws_size,
                              hipStream_t stream) {
  Args a;
  a.x = (const char*)d_in[0]; a.kc = (const char*)d_in[1]; a.vc = (const char*)d_in[2]; a.wq = (const char*)d_in[3]; a.wo = (const char*)d_in[4];
  a.wg = (const char*)d_in[6]; a.wu = (const char*)d_in[7]; a.wd = (const char*)d_in[8];
  a.out = (char*)d_out; a.ws = (char*)d_ws;
  fused_decode<<<256, 576, 0, stream>>>(a);
}
```

```cpp
#define WAUX 0
#define KVAUX 0
#include <hip/hip_runtime.h>
#include <stdint.h>

typedef unsigned int u32;
typedef unsigned long long u64;
typedef u32 v4u __attribute__((ext_vector_type(4)));
typedef u32 v2u __attribute__((ext_vector_type(2)));
typedef float v4f __attribute__((ext_vector_type(4)));
typedef float v2f __attribute__((ext_vector_type(2)));
typedef short v8s __attribute__((ext_vector_type(8)));
typedef short v4s __attribute__((ext_vector_type(4)));
typedef __bf16 v2b __attribute__((ext_vector_type(2)));
typedef __amdgpu_buffer_rsrc_t rsrc_t;
#define LAS __attribute__((address_space(3)))
#define GAS __attribute__((address_space(1)))

#define QPOS 12
#define FFN_DELAY 0
#define WO_DELAY 0
#define TOUCH 0
#define EARLY 5
#define FFN_EARLY 0
#define GAP 1
#define LOADGAP() do { __builtin_amdgcn_sched_barrier(0); asm volatile("s_nop 0"); __builtin_amdgcn_sched_barrier(0); } while (0)
#define CODE_PF 119

constexpr int SK = 32768, WIN = 4096;
constexpr u32 SENT = 0xFFFFFFFFu, OOB = 0x80000000u;
constexpr u32 SPIN_CAP = 1u << 18;
constexpr u32 WS_EPOCH = 0;
constexpr u32 WS_QG = 8192;
constexpr u32 WS_OB = 393216, BOX = 1152;
constexpr u32 WS_HB = 802816;
constexpr u32 WS_HID = 819200;
constexpr u32 WS_BYTES = 1u << 20;
constexpr int L_STG = 0;
constexpr int L_WQ = 65536;
constexpr int L_ORED = 81920;
constexpr int L_P = 90112;
constexpr int L_QP = 92160;
constexpr int L_LW = 94208;
constexpr int L_FQ = 94272;
constexpr int L_Q = 94336;
constexpr int L_H = 95360;
constexpr int L_BYTES = 95520 + 128;

struct Args {
  const char *x, *kc, *vc, *wq, *wo, *wg, *wu, *wd;
  char *out, *ws;
};

#define NWAVES 8
#define STAMP(i) do {} while (0)
#define STAMP_OUT() do {} while (0)

__device__ __forceinline__ rsrc_t mkrsrc(const void* p, u32 bytes) { return __builtin_amdgcn_make_buffer_rsrc((void*)p, 0, bytes, 0x00020000); }
__device__ __forceinline__ v4u ld_nt(rsrc_t r, u32 voff, u32 soff) { return __builtin_amdgcn_raw_buffer_load_b128(r, voff, soff, KVAUX); }
__device__ __forceinline__ v4u ld_dev(rsrc_t r, u32 off) { return __builtin_amdgcn_raw_buffer_load_b128(r, off, 0, 16); }
__device__ __forceinline__ void st_dev(rsrc_t r, u32 off, v4u v) { __builtin_amdgcn_raw_buffer_store_b128(v, r, off, 0, 16); }
__device__ __forceinline__ void st_dev32(rsrc_t r, u32 off, u32 v) { __builtin_amdgcn_raw_buffer_store_b32(v, r, off, 0, 16); }
__device__ __forceinline__ void st_dev64(rsrc_t r, u32 off, v2u v) { __builtin_amdgcn_raw_buffer_store_b64(v, r, off, 0, 16); }
template <int AUX = 2> __device__ __forceinline__ void dma16(rsrc_t r, LAS char* dst, u32 voff, u32 soff) { __builtin_amdgcn_raw_ptr_buffer_load_lds(r, (LAS void*)dst, 16, voff, soff, 0, AUX); }
__device__ __forceinline__ float asf(u32 x) { return __builtin_bit_cast(float, x); }
__device__ __forceinline__ u32 asu(float x) { return __builtin_bit_cast(u32, x); }
__device__ __forceinline__ u32 pk2(float lo, float hi) { v2f f = {lo, hi}; return __builtin_bit_cast(u32, __builtin_convertvector(f, v2b)); }
__device__ __forceinline__ float bflo(u32 x) { return asf(x << 16); }
__device__ __forceinline__ float bfhi(u32 x) { return asf(x & 0xffff0000u); }
__device__ __forceinline__ float bfr(float x) { return bflo(pk2(x, x)); }
template <int CTRL> __device__ __forceinline__ float dppf(float x) { return asf((u32)__builtin_amdgcn_mov_dpp((int)asu(x), CTRL, 0xf, 0xf, true)); }
template <int CTRL> __device__ __forceinline__ u32 dppu(u32 x) { return (u32)__builtin_amdgcn_mov_dpp((int)x, CTRL, 0xf, 0xf, true); }
constexpr int XOR1 = 0xB1, XOR2 = 0x4E, HMIR = 0x141, ROR8 = 0x128;
__device__ __forceinline__ float fmx(float a, float b) { float r; asm("v_max_f32 %0, %1, %2" : "=v"(r) : "v"(a), "v"(b)); return r; }
__device__ __forceinline__ float fmx3(float a, float b, float c) { float r; asm("v_max3_f32 %0, %1, %2, %3" : "=v"(r) : "v"(a), "v"(b), "v"(c)); return r; }
__device__ __forceinline__ float fold32(float a, float b) { auto s = __builtin_amdgcn_permlane32_swap(asu(a), asu(b), false, false); return asf(s[0]) + asf(s[1]); }
__device__ __forceinline__ float fold16(float a, float b) { auto s = __builtin_amdgcn_permlane16_swap(asu(a), asu(b), false, false); return asf(s[0]) + asf(s[1]); }
__device__ __forceinline__ float max32(float a) { auto s = __builtin_amdgcn_permlane32_swap(asu(a), asu(a), false, false); return fmaxf(asf(s[0]), asf(s[1])); }
__device__ __forceinline__ float max16(float a) { auto s = __builtin_amdgcn_permlane16_swap(asu(a), asu(a), false, false); return fmaxf(asf(s[0]), asf(s[1])); }
__device__ __forceinline__ float row_sum(float x) { x += dppf<XOR1>(x); x += dppf<XOR2>(x); x += dppf<HMIR>(x); x += dppf<ROR8>(x); return x; }
__device__ __forceinline__ float wave_sum(float x) { x = row_sum(x); x = fold16(x, x); return fold32(x, x); }
__device__ __forceinline__ v4f mfma(v4u a, v4u b, v4f c) { return __builtin_amdgcn_mfma_f32_16x16x32_bf16(__builtin_bit_cast(v8s, a), __builtin_bit_cast(v8s, b), c, 0, 0, 0); }
__device__ __forceinline__ u32 max4(v4u v) { return max(max(v.x, v.y), max(v.z, v.w)); }
__device__ __forceinline__ v2u tr_read(LAS char* p) { return __builtin_bit_cast(v2u, __builtin_amdgcn_ds_read_tr16_b64_v4i16((LAS v4s*)p)); }
template <int RS> __device__ __forceinline__ v4u bfrag(LAS char* base, int lane) {
  LAS char* p = base + ((lane >> 4) * 8 + ((lane >> 2) & 3)) * RS + (lane & 3) * 8;
  v2u lo = tr_read(p), hi = tr_read(p + 4 * RS);
  return (v4u){lo.x, lo.y, hi.x, hi.y};
}
#define BARRIER() do { asm volatile("s_waitcnt lgkmcnt(0)" ::: "memory"); __builtin_amdgcn_s_barrier(); asm volatile("" ::: "memory"); } while (0)
#define Z4 ((v4f){0.f, 0.f, 0.f, 0.f})
__device__ __forceinline__ float ln_sd(float var) { return bfr(__builtin_amdgcn_sqrtf(bfr(bfr(var) + 1e-5f))); }

template <int N> __device__ __forceinline__ void gather(rsrc_t r, const u32 (&off)[N], v4u (&v)[N]) {
  u32 m, sp = 0;
  do {
    asm volatile("" ::: "memory");
#pragma unroll
    for (int k = 0; k < N; ++k) v[k] = ld_dev(r, off[k]);
    m = 0;
#pragma unroll
    for (int k = 0; k < N; ++k) m = max(m, max4(v[k]));
  } while (m == SENT && ++sp < SPIN_CAP);
}

__global__ void __launch_bounds__(512, 2) fused_decode(Args a) {
  __shared__ __attribute__((aligned(16))) char smem[L_BYTES];
  LAS char* lds = (LAS char*)smem;
  const int w = blockIdx.x, lane = threadIdx.x & 63, wave = __builtin_amdgcn_readfirstlane(threadIdx.x >> 6);
  STAMP(0);
  const int kvh = w & 7, i = w >> 3, b = i >> 4, s = i & 15, g = lane >> 4, c = lane & 15, sw = wave & 3;
  u32 cpf;
  { u64 pc; asm volatile("s_getpc_b64 %0" : "=s"(pc));
    cpf = *(const GAS u32*)(pc + min((s * 8 + wave), CODE_PF - 1) * 128); }
  { const v4u S4 = {SENT, SENT, SENT, SENT};
    *(LAS v4u*)(lds + L_ORED + threadIdx.x * 16) = S4;
    if (threadIdx.x < 264) *(LAS v4u*)(lds + L_P + threadIdx.x * 16) = S4; }
  const rsrc_t ws = mkrsrc(a.ws, WS_BYTES);
  const int role = i < 16 ? 0 : i < 23 ? 1 : i < 27 ? 2 : 3;
  const int gidx = kvh * 7 + (i - 16), didx = kvh * 4 + (i - 23);
  const u32 box = WS_OB + (kvh * 32 + i) * BOX;
  const int cgq = i & 7, kq = i >> 3;
  __builtin_amdgcn_sched_barrier(0);
  const v2u ep = __builtin_amdgcn_raw_buffer_load_b64(ws, WS_EPOCH, 0, 16);
  v4u x0, x1, xf;
  if (wave < 4) {
    x0 = *(const v4u*)(a.x + lane * 32); x1 = *(const v4u*)(a.x + lane * 32 + 16);
    xf = *(const v4u*)(a.x + ((c >> 2) & 1) * 1024 + kq * 256 + sw * 64 + g * 16);
    __builtin_amdgcn_sched_barrier(0);
    const rsrc_t rq = mkrsrc(a.wq, 4u << 20);
    const u32 vo = (lane >> 3) * 8192 + (lane & 7) * 16, so = (kq * 128 + sw * 32) * 8192 + kvh * 1024 + cgq * 128;
#pragma unroll
    for (int j = 0; j < 4; ++j) dma16(rq, lds + L_WQ + sw * 4096 + j * 1024, vo, so + j * 65536);
    const u32 rbox = sw == 0 ? box + 1024 : role == 0 ? WS_HB + (kvh * 16 + i) * 128 : WS_HID + gidx * 128;
    const int nr = sw == 0 ? 17 : (sw == 1 && role < 2) ? 24 : 16;
    st_dev(ws, lane < 16 ? box + sw * 256 + lane * 16 : lane < nr ? rbox + (lane - 16) * 16 : OOB, (v4u){SENT, SENT, SENT, SENT});
  }
  __builtin_amdgcn_sched_barrier(0);
  asm volatile("s_waitcnt lgkmcnt(0)\n\ts_barrier" ::: "memory");
  __builtin_amdgcn_sched_barrier(0);
  STAMP(12);
  const size_t segb = ((size_t)(b * 8 + kvh) * SK + (SK - WIN)) * 256;
  const u32 kvo = s * 65536 + ((sw + i + (i >> 2)) & 3) * 16384 + lane * 16, rj = sw * 5 + i * 3 + kvh * 2;
  int wave2 = wave;
  asm volatile("" : "+s"(wave2));

  if (wave2 < 4) {
    v4u xa;
    { float f[16];
#pragma unroll
      for (int d = 0; d < 4; ++d) { f[2 * d] = bflo(x0[d]); f[2 * d + 1] = bfhi(x0[d]); f[8 + 2 * d] = bflo(x1[d]); f[9 + 2 * d] = bfhi(x1[d]); }
      float sm = 0.f;
#pragma unroll
      for (int e = 0; e < 16; ++e) sm += f[e];
      sm = row_sum(sm);
      const float mean = fold16(sm, sm) * (1.f / 512.f);
      float sq = 0.f;
#pragma unroll
      for (int e = 0; e < 16; ++e) { const float t = f[e] - mean; sq = fmaf(t, t, sq); }
      sq = row_sum(sq);
      const float inv0 = __builtin_amdgcn_rcpf(ln_sd(fold16(sq, sq) * (1.f / 512.f))), mb0 = bfr(mean);
      const bool r1 = c & 4;
      const float mb = r1 ? asf(__builtin_amdgcn_readlane(asu(mb0), 32)) : asf(__builtin_amdgcn_readlane(asu(mb0), 0));
      const float inv = r1 ? asf(__builtin_amdgcn_readlane(asu(inv0), 32)) : asf(__builtin_amdgcn_readlane(asu(inv0), 0));
#pragma unroll
      for (int d = 0; d < 4; ++d) { const u32 t = pk2(bflo(xf[d]) - mb, bfhi(xf[d]) - mb); xa[d] = pk2(bflo(t) * inv, bfhi(t) * inv); }
    }
    STAMP(11);
    asm volatile("s_waitcnt vmcnt(0)" ::: "memory");
    asm volatile("" :: "v"(cpf));
    STAMP(1);
    { v4f qa[4];
#pragma unroll
      for (int nt = 0; nt < 4; ++nt) qa[nt] = mfma(xa, bfrag<128>(lds + L_WQ + sw * 4096 + nt * 32, lane), Z4);
      const float q0 = g < 2 ? qa[0].x : qa[2].x, q1 = g < 2 ? qa[1].x : qa[3].x;
      *(LAS v2u*)(lds + L_QP + sw * 512 + lane * 8) = (v2u){min(asu(q0), SENT - 1u), min(asu(q1), SENT - 1u)}; }
    if (sw == 0) {
      v2u t[4]; u32 sp = 0, m;
      do {
        asm volatile("" ::: "memory");
#pragma unroll
        for (int u = 0; u < 4; ++u) t[u] = *(LAS v2u*)(lds + L_QP + u * 512 + lane * 8);
        m = max(max(max(t[0].x, t[0].y), max(t[1].x, t[1].y)), max(max(t[2].x, t[2].y), max(t[3].x, t[3].y)));
      } while (__any(m == SENT) && ++sp < SPIN_CAP);
      const float q0 = (asf(t[0].x) + asf(t[1].x)) + (asf(t[2].x) + asf(t[3].x)), q1 = (asf(t[0].y) + asf(t[1].y)) + (asf(t[2].y) + asf(t[3].y));
      const u64 tag = (((u64)__builtin_amdgcn_readfirstlane(ep.y) << 32) | (u64)(u32)__builtin_amdgcn_readfirstlane(ep.x)) + 1ull;
      const u32 thi = (u32)(tag >> 8), tlo = (u32)tag << 24;
      st_dev(ws, WS_QG + ((kvh * 2 + (g & 1)) * 4 + kq) * 4096 + cgq * 512 + ((g >> 1) * 16 + c) * 16,
             (v4u){((asu(q0) + 0x80u) >> 8) | tlo, thi, ((asu(q1) + 0x80u) >> 8) | tlo, thi});
    }
    STAMP(2);
    __builtin_amdgcn_s_barrier();
    __builtin_amdgcn_sched_barrier(0);
    const rsrc_t rv = mkrsrc(a.vc + segb, 1u << 20);
    v4u V[16];
#pragma unroll
    for (int j = 0; j < 16; ++j) { V[j] = ld_nt(rv, kvo, ((j + rj) & 15) * 1024); LOADGAP(); }
    __builtin_amdgcn_sched_barrier(0);
    __builtin_amdgcn_s_setprio(3);
    STAMP(3);
    v4u pf0, pf1; v4f cw;
    { v2u pp; v4u l4; u32 sp = 0;
      do { asm volatile("" ::: "memory"); pp = *(LAS v2u*)(lds + L_P + sw * 512 + lane * 8); l4 = *(LAS v4u*)(lds + L_LW + sw * 16);
           if (!__any(max(max(pp.x, pp.y), max4(l4)) == SENT)) break;
           __builtin_amdgcn_s_sleep(1);
      } while (++sp < SPIN_CAP);
      pf0 = (v4u){dppu<0x00>(pp.x), dppu<0x00>(pp.y), dppu<0x55>(pp.x), dppu<0x55>(pp.y)};
      pf1 = (v4u){dppu<0xAA>(pp.x), dppu<0xAA>(pp.y), dppu<0xFF>(pp.x), dppu<0xFF>(pp.y)};
      const float m = fmx(fmx3(asf(l4.x), asf(l4.y), asf(l4.z)), asf(l4.w));
#pragma unroll
      for (int u = 0; u < 4; ++u) cw[u] = __builtin_amdgcn_exp2f(asf(l4[u]) - m);
      cw *= __builtin_amdgcn_rcpf((cw.x + cw.y) + (cw.z + cw.w)); }
    LAS char* rp = lds + L_ORED + (lane & 3) * 512 + (sw * 16 + (lane >> 2)) * 8;
    u32 oaddr = (lane & 3) == 0 ? box + sw * 256 + lane * 4 : OOB;
    asm volatile("" : "+v"(oaddr), "+v"(rp), "+v"(cw));
    STAMP(4);
    v4f oa[8];
#pragma unroll
    for (int G = 0; G < 2; ++G) {
      v4u vt[8];
#pragma unroll
      for (int m = 0; m < 4; ++m) {
#pragma unroll
        for (int d = 0; d < 4; ++d) {
          vt[2 * d][m] = __builtin_amdgcn_perm(V[8 * G + 2 * m + 1][d], V[8 * G + 2 * m][d], 0x05040100u);
          vt[2 * d + 1][m] = __builtin_amdgcn_perm(V[8 * G + 2 * m + 1][d], V[8 * G + 2 * m][d], 0x07060302u);
        }
        if (4 * G + m == EARLY) *(LAS u32*)(lds + L_FQ + 16 + sw * 4) = 1u;
        if (m < 3) __builtin_amdgcn_sched_barrier(0);
      }
#pragma unroll
      for (int e = 0; e < 8; ++e) oa[e] = mfma(G ? pf1 : pf0, vt[e], G ? oa[e] : Z4);
      __builtin_amdgcn_sched_barrier(0);
    }
    STAMP(5);
#pragma unroll
    for (int e = 0; e < 8; e += 2) *(LAS v2f*)(lds + L_ORED + sw * 2048 + e * 256 + lane * 8) = (v2f){oa[e].x, oa[e + 1].x};
    { v2u t[4]; u32 sp = 0, m;
      do {
        asm volatile("" ::: "memory");
#pragma unroll
        for (int u = 0; u < 4; ++u) t[u] = *(LAS v2u*)(rp + u * 2048);
        m = max(max(max(t[0].x, t[0].y), max(t[1].x, t[1].y)), max(max(t[2].x, t[2].y), max(t[3].x, t[3].y)));
      } while (__any(m == SENT) && ++sp < (1u << 14));
      const float o0 = fmaf(asf(t[0].x), cw.x, asf(t[1].x) * cw.y) + fmaf(asf(t[2].x), cw.z, asf(t[3].x) * cw.w);
      const float o1 = fmaf(asf(t[0].y), cw.x, asf(t[1].y) * cw.y) + fmaf(asf(t[2].y), cw.z, asf(t[3].y) * cw.w);
      const u32 ob = min(pk2(o0, o1), SENT - 1u);
      st_dev(ws, oaddr, (v4u){ob, dppu<0x55>(ob), dppu<0xAA>(ob), dppu<0xFF>(ob)});
    }
    __builtin_amdgcn_sched_barrier(0);
    *(LAS u32*)(lds + L_FQ + 32 + sw * 4) = 1u;
    STAMP(6);
    STAMP_OUT();
    return;
  }

  const rsrc_t rk = mkrsrc(a.kc + segb, 1u << 20);
  v4u K[16], qg[4];
#pragma unroll
  for (int j = 0; j < 16; ++j) { K[j] = ld_nt(rk, kvo, ((j + rj) & 15) * 1024); LOADGAP(); }
  __builtin_amdgcn_sched_barrier(0);
  STAMP(11);
  __builtin_amdgcn_s_barrier();
  __builtin_amdgcn_s_setprio(2);
  LAS char* stg = lds + L_STG + sw * 16384;
  const u32 qo = WS_QG + (kvh * 2 + b) * 16384 + sw * 1024 + lane * 16;
#pragma unroll
  for (int j = 0; j <= 16; ++j) {
    if (j == QPOS) {
      __builtin_amdgcn_sched_barrier(0);
#pragma unroll
      for (int k = 0; k < 4; ++k) qg[k] = ld_dev(ws, qo + k * 4096);
      __builtin_amdgcn_sched_barrier(0);
    }
    if (j < 16) *(LAS v4u*)(stg + j * 1024 + g * 256 + ((c ^ (4 * g + (j & 3))) * 16)) = K[j];
  }
  asm volatile("" :: "v"(cpf));
  STAMP(1);
  v4u kf[4][4];
#pragma unroll
  for (int t = 0; t < 4; ++t)
#pragma unroll
    for (int kk = 0; kk < 4; ++kk) kf[t][kk] = *(LAS v4u*)(stg + (4 * t + (c & 3)) * 1024 + (c >> 2) * 256 + (((4 * kk + g) ^ c) * 16));
  const u64 tag = (((u64)__builtin_amdgcn_readfirstlane(ep.y) << 32) | (u64)(u32)__builtin_amdgcn_readfirstlane(ep.x)) + 1ull;
  const u32 thi = (u32)(tag >> 8), tlo = (u32)tag << 24;
  for (u32 sp = 0;; ++sp) {
    u32 bh = 0, bl = 0;
#pragma unroll
    for (int k = 0; k < 4; ++k) { bh |= (qg[k].y ^ thi) | (qg[k].w ^ thi); bl |= (qg[k].x ^ tlo) | (qg[k].z ^ tlo); }
    if (!__any((bh | (bl >> 24)) != 0u) || sp >= SPIN_CAP) break;
    asm volatile("" ::: "memory");
#pragma unroll
    for (int k = 0; k < 4; ++k) qg[k] = ld_dev(ws, qo + k * 4096);
  }
  { const float c0 = (asf(qg[0].x << 8) + asf(qg[1].x << 8)) + (asf(qg[2].x << 8) + asf(qg[3].x << 8));
    const float c1 = (asf(qg[0].z << 8) + asf(qg[1].z << 8)) + (asf(qg[2].z << 8) + asf(qg[3].z << 8));
    const u32 qq = pk2(c0, c1);
    LAS unsigned short* qp = (LAS unsigned short*)(lds + L_Q + sw * 256 + g * 64 + c * 2);
    qp[0] = (unsigned short)qq; qp[16] = (unsigned short)(qq >> 16);
    asm volatile("s_waitcnt lgkmcnt(0)" ::: "memory");
    *(LAS u32*)(lds + L_FQ + sw * 4) = 1u;
    STAMP(2);
    u32 sp = 0; v4u f;
    do { asm volatile("" ::: "memory"); f = *(LAS v4u*)(lds + L_FQ); } while (max4(f) == SENT && ++sp < SPIN_CAP);
    asm volatile("" ::: "memory"); }
  STAMP(3);
  v4f sc[4] = {Z4, Z4, Z4, Z4};
#pragma unroll
  for (int kk = 0; kk < 4; ++kk) {
    const v4u qf = *(LAS v4u*)(lds + L_Q + (c >> 2) * 256 + (4 * kk + g) * 16);
#pragma unroll
    for (int t = 0; t < 4; ++t) sc[t] = mfma(kf[t][kk], qf, sc[t]);
  }
  { float p[4];
    const bool u1 = c & 1, u2 = c & 2;
#pragma unroll
    for (int r = 0; r < 4; ++r) { const float lo = u1 ? sc[1][r] : sc[0][r], hi = u1 ? sc[3][r] : sc[2][r]; p[r] = u2 ? hi : lo; }
#pragma unroll
    for (int r = 0; r < 4; r += 2) {
      const u32 t = pk2(p[r], p[r + 1]);
      const u32 u = pk2(bflo(t) * 0.08838834764831845f, bfhi(t) * 0.08838834764831845f);
      p[r] = bflo(u); p[r + 1] = bfhi(u);
    }
    float mx = fmx(fmx3(p[0], p[1], p[2]), p[3]);
    mx = fmaxf(mx, dppf<XOR1>(mx)); mx = fmaxf(mx, dppf<XOR2>(mx));
    mx = max32(max16(mx));
    float ls = 0.f;
#pragma unroll
    for (int r = 0; r < 4; ++r) { p[r] = __builtin_amdgcn_exp2f((p[r] - mx) * 1.4426950408889634f); ls += p[r]; }
    ls += dppf<XOR1>(ls); ls += dppf<XOR2>(ls);
    ls = fold16(ls, ls); ls = fold32(ls, ls);
    const float rls = __builtin_amdgcn_rcpf(ls);
    *(LAS v2u*)(lds + L_P + sw * 512 + lane * 8) = (v2u){min(pk2(p[0] * rls, p[1] * rls), SENT - 1u), min(pk2(p[2] * rls, p[3] * rls), SENT - 1u)};
    if (lane < 16) *(LAS u32*)(lds + L_LW + (c >> 2) * 16 + sw * 4) = min(asu(fmaf(mx, 1.4426950408889634f, __builtin_amdgcn_logf(ls))), SENT - 1u); }
  if (sw == 0) {
    v4u l4; u32 sp = 0;
    do { asm volatile("" ::: "memory"); l4 = *(LAS v4u*)(lds + L_LW + (lane & 3) * 16); } while (__any(max4(l4) == SENT) && ++sp < SPIN_CAP);
    const float m = fmx(fmx3(asf(l4.x), asf(l4.y), asf(l4.z)), asf(l4.w));
    const float sm = (__builtin_amdgcn_exp2f(asf(l4.x) - m) + __builtin_amdgcn_exp2f(asf(l4.y) - m)) + (__builtin_amdgcn_exp2f(asf(l4.z) - m) + __builtin_amdgcn_exp2f(asf(l4.w) - m));
    const u32 lb = min(asu(m + __builtin_amdgcn_logf(sm)), SENT - 1u);
    st_dev(ws, lane == 0 ? box + 1024 : OOB, (v4u){lb, dppu<0x55>(lb), dppu<0xAA>(lb), dppu<0xFF>(lb)});
  }
  STAMP(4);
  __builtin_amdgcn_s_setprio(0);
  auto wait_flags = [&](int off) { u32 sp = 0; v4u f;
#pragma unroll 1
    do { asm volatile("" ::: "memory"); f = *(LAS v4u*)(lds + L_FQ + off); } while (max4(f) == SENT && ++sp < SPIN_CAP);
    asm volatile("" ::: "memory"); };

  if (role == 0) {
    { const rsrc_t r = mkrsrc(a.wo, 4u << 20);
      const u32 vo = (lane >> 2) * 1024 + (lane & 3) * 16, so = (kvh * 512 + sw * 128) * 1024 + i * 64;
      wait_flags(16);
#pragma unroll
      for (int k = 0; k < 4; ++k) { const int j = k + (i & 1) * 4; dma16<WAUX>(r, stg + j * 1024, vo, so + j * 16384); }
      wait_flags(32);
#pragma unroll
      for (int k = 0; k < 4; ++k) { const int j = k + 4 - (i & 1) * 4; dma16<WAUX>(r, stg + j * 1024, vo, so + j * 16384); } }
    STAMP(5);
    v4f wt[2];
    { u32 lw, sp = 0;
      do { asm volatile("" ::: "memory"); lw = __builtin_amdgcn_raw_buffer_load_b32(ws, WS_OB + (kvh * 32 + (lane & 31)) * BOX + 1024 + sw * 4, 0, 16); } while (lw == SENT && ++sp < SPIN_CAP);
      float m = asf(lw);
      m = fmaxf(m, dppf<XOR1>(m)); m = fmaxf(m, dppf<XOR2>(m)); m = fmaxf(m, dppf<HMIR>(m)); m = fmaxf(m, dppf<ROR8>(m));
      const float e = __builtin_amdgcn_exp2f(asf(lw) - m);
      *(LAS float*)(lds + L_WQ + 4096 + sw * 256 + lane * 4) = e * __builtin_amdgcn_rcpf(row_sum(e));
      wt[0] = *(LAS v4f*)(lds + L_WQ + 4096 + sw * 256 + g * 16); wt[1] = *(LAS v4f*)(lds + L_WQ + 4096 + sw * 256 + 64 + g * 16); }
    asm volatile("s_waitcnt vmcnt(0)" ::: "memory");
    STAMP(7);
    v4u bf[4][2];
#pragma unroll
    for (int ks = 0; ks < 4; ++ks)
#pragma unroll
      for (int nt = 0; nt < 2; ++nt) bf[ks][nt] = bfrag<64>(stg + ks * 2048 + nt * 32, lane);
    STAMP(8);
    v4u af[8];
    { u32 off[8];
#pragma unroll
      for (int k = 0; k < 8; ++k) off[k] = WS_OB + (kvh * 32 + c) * BOX + sw * 256 + g * 16 + (k >> 2) * (16 * BOX) + (k & 3) * 64;
      gather<8>(ws, off, af); }
    STAMP(9);
    float val[2][2];
#pragma unroll
    for (int r = 0; r < 2; ++r)
#pragma unroll
      for (int nt = 0; nt < 2; ++nt) {
        v4f d = Z4;
#pragma unroll
        for (int ks = 0; ks < 4; ++ks) d = mfma(af[4 * r + ks], bf[ks][nt], d);
        val[r][nt] = fmaf(d.x, wt[r][0], d.y * wt[r][1]) + fmaf(d.z, wt[r][2], d.w * wt[r][3]);
      }
    const float y = fold16(fold32(val[0][0], val[1][0]), fold32(val[0][1], val[1][1]));
    *(LAS float*)(lds + L_WQ + sw * 256 + lane * 4) = y;
    BARRIER();
    if (sw == 0) {
      v2f t[4];
#pragma unroll
      for (int u = 0; u < 4; ++u) t[u] = *(LAS v2f*)(lds + L_WQ + u * 256 + (lane & 31) * 8);
      const v2f o2 = (t[0] + t[1]) + (t[2] + t[3]);
      st_dev32(ws, lane < 32 ? WS_HB + (kvh * 16 + i) * 128 + lane * 4 : OOB, min(pk2(o2.x, o2.y), SENT - 1u));
    }
    STAMP(10);
    STAMP_OUT();
    return;
  }

  v4u xr[4];
  if (role < 3) {
#pragma unroll
    for (int ks = 0; ks < 4; ++ks) xr[ks] = *(const v4u*)(a.x + (role == 1 ? (c >> 3) * 1024 + sw * 256 + ks * 64 + g * 16 : (sw & 1) * 1024 + lane * 16));
    u32 bad, sp = 0;
    do {
      asm volatile("" ::: "memory");
      const v2u r = __builtin_amdgcn_raw_buffer_load_b64(ws, WS_QG + (sw * 2 + (lane >> 5)) * 32768 + (lane & 31) * 512, 0, 16);
      bad = (r.y ^ thi) | ((r.x ^ tlo) >> 24);
    } while (bad != 0u && ++sp < SPIN_CAP);
  }
  wait_flags(FFN_EARLY ? 16 : 32);
  if (FFN_DELAY) __builtin_amdgcn_s_sleep(FFN_DELAY);
  if (role == 3) { STAMP_OUT(); return; }
  if (role == 1) {
    const u32 vo = (lane >> 2) * 3584 + (lane & 3) * 16, so = sw * 128 * 3584 + gidx * 64;
    const int m0 = gidx & 1;
    const rsrc_t r0 = mkrsrc(m0 ? a.wu : a.wg, 512 * 3584), r1 = mkrsrc(m0 ? a.wg : a.wu, 512 * 3584);
#pragma unroll
    for (int j = 0; j < 8; ++j) dma16<WAUX>(r0, stg + m0 * 8192 + j * 1024, vo, so + j * 57344);
#pragma unroll
    for (int j = 0; j < 8; ++j) dma16<WAUX>(r1, stg + (m0 ^ 1) * 8192 + j * 1024, vo, so + j * 57344);
  } else {
    const rsrc_t r = mkrsrc(a.wd, 1792 * 1024);
    const u32 vo = (lane >> 1) * 1024 + (lane & 1) * 16, so = sw * 14 * 32768 + didx * 32;
    int j = ((i - 23) * 7) >> 1;
#pragma unroll
    for (int k = 0; k < 14; ++k) { dma16<WAUX>(r, stg + j * 1024, vo, so + j * 32768); j = j == 13 ? 0 : j + 1; }
  }
  STAMP(5);

  if (role == 1) {
    v4u xa[4];
#pragma unroll
    for (int ks = 0; ks < 4; ++ks) xa[ks] = (c & 7) ? (v4u){0u, 0u, 0u, 0u} : xr[ks];
    asm volatile("s_waitcnt vmcnt(0)" ::: "memory");
    STAMP(7);
    v4u bf[4][4];
#pragma unroll
    for (int ks = 0; ks < 4; ++ks)
#pragma unroll
      for (int q = 0; q < 4; ++q) bf[ks][q] = bfrag<64>(stg + (q >> 1) * 8192 + ks * 2048 + (q & 1) * 32, lane);
    const v4u ones = {0x3f803f80u, 0x3f803f80u, 0x3f803f80u, 0x3f803f80u};
    v4f ac[5] = {Z4, Z4, Z4, Z4, Z4}, cs[4] = {Z4, Z4, Z4, Z4};
#pragma unroll
    for (int ks = 0; ks < 4; ++ks) {
#pragma unroll
      for (int q = 0; q < 4; ++q) { ac[q] = mfma(xa[ks], bf[ks][q], ac[q]); cs[q] = mfma(ones, bf[ks][q], cs[q]); }
      ac[4] = mfma(xa[ks], ones, ac[4]);
    }
    *(LAS v2f*)(lds + L_WQ + 8192 + sw * 512 + lane * 8) = (g & 1) ? (v2f){cs[1].x, cs[3].x} : (v2f){cs[0].x, cs[2].x};
    BARRIER();
    v2f csum;
    { v2f t[4];
#pragma unroll
      for (int u = 0; u < 4; ++u) t[u] = *(LAS v2f*)(lds + L_WQ + 8192 + u * 512 + lane * 8);
      csum = (t[0] + t[1]) + (t[2] + t[3]); }
    STAMP(8);
    v4u af[4];
    { const u32 o0 = WS_HB + ((c & 7) * 16 + sw * 4) * 128 + (c >> 3) * 64 + g * 16; const u32 off[4] = {o0, o0 + 128, o0 + 256, o0 + 384}; gather<4>(ws, off, af); }
    STAMP(9);
    float r5[5];
#pragma unroll
    for (int q = 0; q < 5; ++q) {
      v4f d = ac[q];
#pragma unroll
      for (int ks = 0; ks < 4; ++ks) d = mfma(af[ks], q < 4 ? bf[ks][q] : ones, d);
      r5[q] = (d.x + d.y) + (d.z + d.w);
    }
    *(LAS v4f*)(lds + L_WQ + sw * 1024 + lane * 16) = (v4f){fold16(r5[0], r5[1]), fold16(r5[2], r5[3]), fold16(r5[4], r5[4]), 0.f};
    BARRIER();
    if (sw == 0) {
      v4f t[4];
#pragma unroll
      for (int u = 0; u < 4; ++u) t[u] = *(LAS v4f*)(lds + L_WQ + u * 1024 + lane * 16);
      const v4f z = (t[0] + t[1]) + (t[2] + t[3]);
      const float mb = bfr(z.z * (1.f / 512.f));
      const float hid = fmaf(-mb, csum.x, z.x) * fmaf(-mb, csum.y, z.y);
      const u32 hb = min(pk2(hid, dppf<XOR1>(hid)), SENT - 1u);
      st_dev32(ws, (lane & 1) == 0 ? WS_HID + gidx * 128 + lane * 2 : OOB, hb);
    }
    STAMP(10);
    STAMP_OUT();
    return;
  }

  {
    asm volatile("s_waitcnt vmcnt(0)" ::: "memory");
    __builtin_amdgcn_s_barrier();
    asm volatile("" ::: "memory");
    STAMP(7);
    v4u bf[14];
#pragma unroll
    for (int kb = 0; kb < 14; ++kb) bf[kb] = bfrag<32>(stg + kb * 1024, lane);
    if (sw < 2) {
      v4u hp[8];
      { u32 off[8];
#pragma unroll
        for (int u = 0; u < 8; ++u) off[u] = WS_HB + u * 2048 + (lane >> 2) * 128 + sw * 64 + (lane & 3) * 16;
        gather<8>(ws, off, hp); }
      float h[8], sm = 0.f;
#pragma unroll
      for (int d = 0; d < 4; ++d) {
        float lo = 0.f, hi = 0.f;
#pragma unroll
        for (int u = 0; u < 8; ++u) { lo += bflo(hp[u][d]); hi += bfhi(hp[u][d]); }
        const u32 t = pk2(lo, hi), hh = pk2(bflo(xr[0][d]) + bflo(t), bfhi(xr[0][d]) + bfhi(t));
        h[2 * d] = bflo(hh); h[2 * d + 1] = bfhi(hh);
        sm += h[2 * d] + h[2 * d + 1];
      }
      const float mean = wave_sum(sm) * (1.f / 512.f);
      float sq = 0.f;
#pragma unroll
      for (int e = 0; e < 8; ++e) { const float t = h[e] - mean; sq = fmaf(t, t, sq); }
      const float inv = __builtin_amdgcn_rcpf(ln_sd(wave_sum(sq) * (1.f / 512.f)));
      if ((lane >> 1) == didx) {
        *(LAS v4f*)(lds + L_H + sw * 64 + (lane & 1) * 32) = (v4f){h[0], h[1], h[2], h[3]};
        *(LAS v4f*)(lds + L_H + sw * 64 + (lane & 1) * 32 + 16) = (v4f){h[4], h[5], h[6], h[7]};
      }
      if (lane == 0) *(LAS float*)(lds + L_H + 128 + sw * 4) = inv * inv;
    }
    STAMP(8);
    v4u af[2];
    { const u32 o0 = (c & 7) < 7 ? WS_HID + (sw * 14 + (c & 7)) * 128 + (c >> 3) * 64 + g * 16 : OOB; const u32 off[2] = {o0, o0 + 896}; gather<2>(ws, off, af); }
    STAMP(9);
    v4f d[7];
#pragma unroll
    for (int kb = 0; kb < 7; ++kb) d[kb] = mfma(af[1], bf[7 + kb], mfma(af[0], bf[kb], Z4));
    const bool odd = g & 1;
    float y = ((odd ? d[4].x : d[0].x) + (odd ? d[5].y : d[1].y)) + ((odd ? d[6].z : d[2].z) + (odd ? 0.f : d[3].w));
    y = fold16(y, y);
    *(LAS float*)(lds + L_WQ + sw * 256 + lane * 4) = y;
    BARRIER();
    if (sw == 0) {
      if (lane < 16) {
        const int r = lane >> 3, cp = lane & 7;
        v2f t[4];
#pragma unroll
        for (int u = 0; u < 4; ++u) t[u] = *(LAS v2f*)(lds + L_WQ + u * 256 + r * 128 + cp * 8);
        const v2f z = (t[0] + t[1]) + (t[2] + t[3]);
        const v2f hh = *(LAS v2f*)(lds + L_H + r * 64 + cp * 8);
        const float sc2 = *(LAS float*)(lds + L_H + 128 + r * 4);
        const u32 f2 = pk2(z.x * sc2, z.y * sc2);
        *(u32*)(a.out + r * 1024 + didx * 32 + cp * 4) = pk2(hh.x + bflo(f2), hh.y + bfhi(f2));
      }
      st_dev64(ws, (w == 23 * 8 && lane == 0) ? WS_EPOCH : OOB, (v2u){(u32)tag, (u32)(tag >> 32)});
    }
    STAMP(10);
    STAMP_OUT();
  }
}

extern "C" void kernel_launch(void* const* d_in, const int* in_sizes, int n_in, void* d_out, int out_size, void* d_ws, size_t ws_size,
                              hipStream_t stream) {
  Args a;
  a.x = (const char*)d_in[0]; a.kc = (const char*)d_in[1]; a.vc = (const char*)d_in[2]; a.wq = (const char*)d_in[3]; a.wo = (const char*)d_in[4];
  a.wg = (const char*)d_in[6]; a.wu = (const char*)d_in[7]; a.wd = (const char*)d_in[8];
  a.out = (char*)d_out; a.ws = (char*)d_ws;
  fused_decode<<<256, 512, 0, stream>>>(a);
}
```

```cpp
#define CODE_PF 123
#define EXFIL 1
#define WAUX 0
#define KVAUX 0
#include <hip/hip_runtime.h>
#include <stdint.h>

typedef unsigned int u32;
typedef unsigned long long u64;
typedef u32 v4u __attribute__((ext_vector_type(4)));
typedef u32 v2u __attribute__((ext_vector_type(2)));
typedef float v4f __attribute__((ext_vector_type(4)));
typedef float v2f __attribute__((ext_vector_type(2)));
typedef short v8s __attribute__((ext_vector_type(8)));
typedef short v4s __attribute__((ext_vector_type(4)));
typedef __bf16 v2b __attribute__((ext_vector_type(2)));
typedef __amdgpu_buffer_rsrc_t rsrc_t;
#define LAS __attribute__((address_space(3)))
#define GAS __attribute__((address_space(1)))

#define QPOS 12
#define FFN_DELAY 0
#define WO_DELAY 0
#define TOUCH 0
#define EARLY 5
#define FFN_EARLY 0
#define EXW 184
#define GAP 1
#define LOADGAP() do { __builtin_amdgcn_sched_barrier(0); asm volatile("s_nop 0"); __builtin_amdgcn_sched_barrier(0); } while (0)

constexpr int SK = 32768, WIN = 4096;
constexpr u32 SENT = 0xFFFFFFFFu, OOB = 0x80000000u;
constexpr u32 SPIN_CAP = 1u << 18;
constexpr u32 WS_EPOCH = 0;
constexpr u32 WS_QG = 8192;
constexpr u32 WS_OB = 393216, BOX = 1152;
constexpr u32 WS_HB = 802816;
constexpr u32 WS_HID = 819200;
constexpr u32 WS_BYTES = 1u << 20;
constexpr int L_STG = 0;
constexpr int L_WQ = 65536;
constexpr int L_ORED = 81920;
constexpr int L_P = 90112;
constexpr int L_QP = 92160;
constexpr int L_LW = 94208;
constexpr int L_FQ = 94272;
constexpr int L_Q = 94336;
constexpr int L_H = 95360;
constexpr int L_BYTES = 95520 + 128;

struct Args {
  const char *x, *kc, *vc, *wq, *wo, *wg, *wu, *wd;
  char *out, *ws;
};

#define NWAVES 8
#define STAMP(i) do { if ((i) == 0 || (i) == (EXFIL & 15)) { __builtin_amdgcn_sched_barrier(0); ts[i] = __builtin_amdgcn_s_memrealtime(); __builtin_amdgcn_sched_barrier(0); if (EXFIL >= 16 && (i) != 0 && wave == 0 && lane == 0) *(volatile LAS u64*)(lds + L_BYTES - 128 + (i) * 8) = ts[i]; } } while (0)
#define STAMP_OUT() do { if (w == EXW && wave == 4) { asm volatile("s_waitcnt lgkmcnt(0)" ::: "memory"); u64 tk = EXFIL < 16 ? ts[EXFIL & 15] : *(volatile LAS u64*)(lds + L_BYTES - 128 + (EXFIL & 15) * 8); u64 until = ts[0] + 4000 + 10 * (tk - ts[0]); while (__builtin_amdgcn_s_memrealtime() < until) __builtin_amdgcn_s_sleep(2); } } while (0)

__device__ __forceinline__ rsrc_t mkrsrc(const void* p, u32 bytes) { return __builtin_amdgcn_make_buffer_rsrc((void*)p, 0, bytes, 0x00020000); }
__device__ __forceinline__ v4u ld_nt(rsrc_t r, u32 voff, u32 soff) { return __builtin_amdgcn_raw_buffer_load_b128(r, voff, soff, KVAUX); }
__device__ __forceinline__ v4u ld_dev(rsrc_t r, u32 off) { return __builtin_amdgcn_raw_buffer_load_b128(r, off, 0, 16); }
__device__ __forceinline__ void st_dev(rsrc_t r, u32 off, v4u v) { __builtin_amdgcn_raw_buffer_store_b128(v, r, off, 0, 16); }
__device__ __forceinline__ void st_dev32(rsrc_t r, u32 off, u32 v) { __builtin_amdgcn_raw_buffer_store_b32(v, r, off, 0, 16); }
__device__ __forceinline__ void st_dev64(rsrc_t r, u32 off, v2u v) { __builtin_amdgcn_raw_buffer_store_b64(v, r, off, 0, 16); }
template <int AUX = 2> __device__ __forceinline__ void dma16(rsrc_t r, LAS char* dst, u32 voff, u32 soff) { __builtin_amdgcn_raw_ptr_buffer_load_lds(r, (LAS void*)dst, 16, voff, soff, 0, AUX); }
__device__ __forceinline__ float asf(u32 x) { return __builtin_bit_cast(float, x); }
__device__ __forceinline__ u32 asu(float x) { return __builtin_bit_cast(u32, x); }
__device__ __forceinline__ u32 pk2(float lo, float hi) { v2f f = {lo, hi}; return __builtin_bit_cast(u32, __builtin_convertvector(f, v2b)); }
__device__ __forceinline__ float bflo(u32 x) { return asf(x << 16); }
__device__ __forceinline__ float bfhi(u32 x) { return asf(x & 0xffff0000u); }
__device__ __forceinline__ float bfr(float x) { return bflo(pk2(x, x)); }
template <int CTRL> __device__ __forceinline__ float dppf(float x) { return asf((u32)__builtin_amdgcn_mov_dpp((int)asu(x), CTRL, 0xf, 0xf, true)); }
template <int CTRL> __device__ __forceinline__ u32 dppu(u32 x) { return (u32)__builtin_amdgcn_mov_dpp((int)x, CTRL, 0xf, 0xf, true); }
constexpr int XOR1 = 0xB1, XOR2 = 0x4E, HMIR = 0x141, ROR8 = 0x128;
__device__ __forceinline__ float fmx(float a, float b) { float r; asm("v_max_f32 %0, %1, %2" : "=v"(r) : "v"(a), "v"(b)); return r; }
__device__ __forceinline__ float fmx3(float a, float b, float c) { float r; asm("v_max3_f32 %0, %1, %2, %3" : "=v"(r) : "v"(a), "v"(b), "v"(c)); return r; }
__device__ __forceinline__ float fold32(float a, float b) { auto s = __builtin_amdgcn_permlane32_swap(asu(a), asu(b), false, false); return asf(s[0]) + asf(s[1]); }
__device__ __forceinline__ float fold16(float a, float b) { auto s = __builtin_amdgcn_permlane16_swap(asu(a), asu(b), false, false); return asf(s[0]) + asf(s[1]); }
__device__ __forceinline__ float max32(float a) { auto s = __builtin_amdgcn_permlane32_swap(asu(a), asu(a), false, false); return fmaxf(asf(s[0]), asf(s[1])); }
__device__ __forceinline__ float max16(float a) { auto s = __builtin_amdgcn_permlane16_swap(asu(a), asu(a), false, false); return fmaxf(asf(s[0]), asf(s[1])); }
__device__ __forceinline__ float row_sum(float x) { x += dppf<XOR1>(x); x += dppf<XOR2>(x); x += dppf<HMIR>(x); x += dppf<ROR8>(x); return x; }
__device__ __forceinline__ float wave_sum(float x) { x = row_sum(x); x = fold16(x, x); return fold32(x, x); }
__device__ __forceinline__ v4f mfma(v4u a, v4u b, v4f c) { return __builtin_amdgcn_mfma_f32_16x16x32_bf16(__builtin_bit_cast(v8s, a), __builtin_bit_cast(v8s, b), c, 0, 0, 0); }
__device__ __forceinline__ u32 max4(v4u v) { return max(max(v.x, v.y), max(v.z, v.w)); }
__device__ __forceinline__ v2u tr_read(LAS char* p) { return __builtin_bit_cast(v2u, __builtin_amdgcn_ds_read_tr16_b64_v4i16((LAS v4s*)p)); }
template <int RS> __device__ __forceinline__ v4u bfrag(LAS char* base, int lane) {
  LAS char* p = base + ((lane >> 4) * 8 + ((lane >> 2) & 3)) * RS + (lane & 3) * 8;
  v2u lo = tr_read(p), hi = tr_read(p + 4 * RS);
  return (v4u){lo.x, lo.y, hi.x, hi.y};
}
#define BARRIER() do { asm volatile("s_waitcnt lgkmcnt(0)" ::: "memory"); __builtin_amdgcn_s_barrier(); asm volatile("" ::: "memory"); } while (0)
#define Z4 ((v4f){0.f, 0.f, 0.f, 0.f})
__device__ __forceinline__ float ln_sd(float var) { return bfr(__builtin_amdgcn_sqrtf(bfr(bfr(var) + 1e-5f))); }

template <int N> __device__ __forceinline__ void gather(rsrc_t r, const u32 (&off)[N], v4u (&v)[N]) {
  u32 m, sp = 0;
  do {
    asm volatile("" ::: "memory");
#pragma unroll
    for (int k = 0; k < N; ++k) v[k] = ld_dev(r, off[k]);
    m = 0;
#pragma unroll
    for (int k = 0; k < N; ++k) m = max(m, max4(v[k]));
  } while (m == SENT && ++sp < SPIN_CAP);
}

__global__ void __launch_bounds__(512, 2) fused_decode(Args a) {
  __shared__ __attribute__((aligned(16))) char smem[L_BYTES];
  LAS char* lds = (LAS char*)smem;
  const int w = blockIdx.x, lane = threadIdx.x & 63, wave = __builtin_amdgcn_readfirstlane(threadIdx.x >> 6);
  u64 ts[16] = {};
  STAMP(0);
  const int kvh = w & 7, i = w >> 3, b = i >> 4, s = i & 15, g = lane >> 4, c = lane & 15, sw = wave & 3;
  u32 cpf;
  { u64 pc; asm volatile("s_getpc_b64 %0" : "=s"(pc));
    cpf = *(const GAS u32*)(pc + min((s * 8 + wave), CODE_PF - 1) * 128); }
  { const v4u S4 = {SENT, SENT, SENT, SENT};
    *(LAS v4u*)(lds + L_ORED + threadIdx.x * 16) = S4;
    if (threadIdx.x < 264) *(LAS v4u*)(lds + L_P + threadIdx.x * 16) = S4; }
  const rsrc_t ws = mkrsrc(a.ws, WS_BYTES);
  const int role = i < 16 ? 0 : i < 23 ? 1 : i < 27 ? 2 : 3;
  const int gidx = kvh * 7 + (i - 16), didx = kvh * 4 + (i - 23);
  const u32 box = WS_OB + (kvh * 32 + i) * BOX;
  const int cgq = i & 7, kq = i >> 3;
  __builtin_amdgcn_sched_barrier(0);
  const v2u ep = __builtin_amdgcn_raw_buffer_load_b64(ws, WS_EPOCH, 0, 16);
  v4u x0, x1, xf;
  if (wave < 4) {
    x0 = *(const v4u*)(a.x + lane * 32); x1 = *(const v4u*)(a.x + lane * 32 + 16);
    xf = *(const v4u*)(a.x + ((c >> 2) & 1) * 1024 + kq * 256 + sw * 64 + g * 16);
    __builtin_amdgcn_sched_barrier(0);
    const rsrc_t rq = mkrsrc(a.wq, 4u << 20);
    const u32 vo = (lane >> 3) * 8192 + (lane & 7) * 16, so = (kq * 128 + sw * 32) * 8192 + kvh * 1024 + cgq * 128;
#pragma unroll
    for (int j = 0; j < 4; ++j) dma16(rq, lds + L_WQ + sw * 4096 + j * 1024, vo, so + j * 65536);
    const u32 rbox = sw == 0 ? box + 1024 : role == 0 ? WS_HB + (kvh * 16 + i) * 128 : WS_HID + gidx * 128;
    const int nr = sw == 0 ? 17 : (sw == 1 && role < 2) ? 24 : 16;
    st_dev(ws, lane < 16 ? box + sw * 256 + lane * 16 : lane < nr ? rbox + (lane - 16) * 16 : OOB, (v4u){SENT, SENT, SENT, SENT});
  }
  __builtin_amdgcn_sched_barrier(0);
  asm volatile("s_waitcnt lgkmcnt(0)\n\ts_barrier" ::: "memory");
  __builtin_amdgcn_sched_barrier(0);
  STAMP(12);
  const size_t segb = ((size_t)(b * 8 + kvh) * SK + (SK - WIN)) * 256;
  const u32 kvo = s * 65536 + ((sw + i + (i >> 2)) & 3) * 16384 + lane * 16, rj = sw * 5 + i * 3 + kvh * 2;
  int wave2 = wave;
  asm volatile("" : "+s"(wave2));

  if (wave2 < 4) {
    v4u xa;
    { float f[16];
#pragma unroll
      for (int d = 0; d < 4; ++d) { f[2 * d] = bflo(x0[d]); f[2 * d + 1] = bfhi(x0[d]); f[8 + 2 * d] = bflo(x1[d]); f[9 + 2 * d] = bfhi(x1[d]); }
      float sm = 0.f;
#pragma unroll
      for (int e = 0; e < 16; ++e) sm += f[e];
      sm = row_sum(sm);
      const float mean = fold16(sm, sm) * (1.f / 512.f);
      float sq = 0.f;
#pragma unroll
      for (int e = 0; e < 16; ++e) { const float t = f[e] - mean; sq = fmaf(t, t, sq); }
      sq = row_sum(sq);
      const float inv0 = __builtin_amdgcn_rcpf(ln_sd(fold16(sq, sq) * (1.f / 512.f))), mb0 = bfr(mean);
      const bool r1 = c & 4;
      const float mb = r1 ? asf(__builtin_amdgcn_readlane(asu(mb0), 32)) : asf(__builtin_amdgcn_readlane(asu(mb0), 0));
      const float inv = r1 ? asf(__builtin_amdgcn_readlane(asu(inv0), 32)) : asf(__builtin_amdgcn_readlane(asu(inv0), 0));
#pragma unroll
      for (int d = 0; d < 4; ++d) { const u32 t = pk2(bflo(xf[d]) - mb, bfhi(xf[d]) - mb); xa[d] = pk2(bflo(t) * inv, bfhi(t) * inv); }
    }
    STAMP(11);
    asm volatile("s_waitcnt vmcnt(0)" ::: "memory");
    asm volatile("" :: "v"(cpf));
    STAMP(1);
    { v4f qa[4];
#pragma unroll
      for (int nt = 0; nt < 4; ++nt) qa[nt] = mfma(xa, bfrag<128>(lds + L_WQ + sw * 4096 + nt * 32, lane), Z4);
      const float q0 = g < 2 ? qa[0].x : qa[2].x, q1 = g < 2 ? qa[1].x : qa[3].x;
      *(LAS v2u*)(lds + L_QP + sw * 512 + lane * 8) = (v2u){min(asu(q0), SENT - 1u), min(asu(q1), SENT - 1u)}; }
    if (sw == 0) {
      v2u t[4]; u32 sp = 0, m;
      do {
        asm volatile("" ::: "memory");
#pragma unroll
        for (int u = 0; u < 4; ++u) t[u] = *(LAS v2u*)(lds + L_QP + u * 512 + lane * 8);
        m = max(max(max(t[0].x, t[0].y), max(t[1].x, t[1].y)), max(max(t[2].x, t[2].y), max(t[3].x, t[3].y)));
      } while (__any(m == SENT) && ++sp < SPIN_CAP);
      const float q0 = (asf(t[0].x) + asf(t[1].x)) + (asf(t[2].x) + asf(t[3].x)), q1 = (asf(t[0].y) + asf(t[1].y)) + (asf(t[2].y) + asf(t[3].y));
      const u64 tag = (((u64)__builtin_amdgcn_readfirstlane(ep.y) << 32) | (u64)(u32)__builtin_amdgcn_readfirstlane(ep.x)) + 1ull;
      const u32 thi = (u32)(tag >> 8), tlo = (u32)tag << 24;
      st_dev(ws, WS_QG + ((kvh * 2 + (g & 1)) * 4 + kq) * 4096 + cgq * 512 + ((g >> 1) * 16 + c) * 16,
             (v4u){((asu(q0) + 0x80u) >> 8) | tlo, thi, ((asu(q1) + 0x80u) >> 8) | tlo, thi});
    }
    STAMP(2);
    __builtin_amdgcn_s_barrier();
    __builtin_amdgcn_sched_barrier(0);
    const rsrc_t rv = mkrsrc(a.vc + segb, 1u << 20);
    v4u V[16];
#pragma unroll
    for (int j = 0; j < 16; ++j) { V[j] = ld_nt(rv, kvo, ((j + rj) & 15) * 1024); LOADGAP(); }
    __builtin_amdgcn_sched_barrier(0);
    __builtin_amdgcn_s_setprio(3);
    STAMP(3);
    v4u pf0, pf1; v4f cw;
    { v2u pp; v4u l4; u32 sp = 0;
      do { asm volatile("" ::: "memory"); pp = *(LAS v2u*)(lds + L_P + sw * 512 + lane * 8); l4 = *(LAS v4u*)(lds + L_LW + sw * 16);
           if (!__any(max(max(pp.x, pp.y), max4(l4)) == SENT)) break;
           __builtin_amdgcn_s_sleep(1);
      } while (++sp < SPIN_CAP);
      pf0 = (v4u){dppu<0x00>(pp.x), dppu<0x00>(pp.y), dppu<0x55>(pp.x), dppu<0x55>(pp.y)};
      pf1 = (v4u){dppu<0xAA>(pp.x), dppu<0xAA>(pp.y), dppu<0xFF>(pp.x), dppu<0xFF>(pp.y)};
      const float m = fmx(fmx3(asf(l4.x), asf(l4.y), asf(l4.z)), asf(l4.w));
#pragma unroll
      for (int u = 0; u < 4; ++u) cw[u] = __builtin_amdgcn_exp2f(asf(l4[u]) - m);
      cw *= __builtin_amdgcn_rcpf((cw.x + cw.y) + (cw.z + cw.w)); }
    LAS char* rp = lds + L_ORED + (lane & 3) * 512 + (sw * 16 + (lane >> 2)) * 8;
    u32 oaddr = (lane & 3) == 0 ? box + sw * 256 + lane * 4 : OOB;
    asm volatile("" : "+v"(oaddr), "+v"(rp), "+v"(cw));
    STAMP(4);
    v4f oa[8];
#pragma unroll
    for (int G = 0; G < 2; ++G) {
      v4u vt[8];
#pragma unroll
      for (int m = 0; m < 4; ++m) {
#pragma unroll
        for (int d = 0; d < 4; ++d) {
          vt[2 * d][m] = __builtin_amdgcn_perm(V[8 * G + 2 * m + 1][d], V[8 * G + 2 * m][d], 0x05040100u);
          vt[2 * d + 1][m] = __builtin_amdgcn_perm(V[8 * G + 2 * m + 1][d], V[8 * G + 2 * m][d], 0x07060302u);
        }
        if (4 * G + m == EARLY) *(LAS u32*)(lds + L_FQ + 16 + sw * 4) = 1u;
        if (m < 3) __builtin_amdgcn_sched_barrier(0);
      }
#pragma unroll
      for (int e = 0; e < 8; ++e) oa[e] = mfma(G ? pf1 : pf0, vt[e], G ? oa[e] : Z4);
      __builtin_amdgcn_sched_barrier(0);
    }
    STAMP(5);
#pragma unroll
    for (int e = 0; e < 8; e += 2) *(LAS v2f*)(lds + L_ORED + sw * 2048 + e * 256 + lane * 8) = (v2f){oa[e].x, oa[e + 1].x};
    { v2u t[4]; u32 sp = 0, m;
      do {
        asm volatile("" ::: "memory");
#pragma unroll
        for (int u = 0; u < 4; ++u) t[u] = *(LAS v2u*)(rp + u * 2048);
        m = max(max(max(t[0].x, t[0].y), max(t[1].x, t[1].y)), max(max(t[2].x, t[2].y), max(t[3].x, t[3].y)));
      } while (__any(m == SENT) && ++sp < (1u << 14));
      const float o0 = fmaf(asf(t[0].x), cw.x, asf(t[1].x) * cw.y) + fmaf(asf(t[2].x), cw.z, asf(t[3].x) * cw.w);
      const float o1 = fmaf(asf(t[0].y), cw.x, asf(t[1].y) * cw.y) + fmaf(asf(t[2].y), cw.z, asf(t[3].y) * cw.w);
      const u32 ob = min(pk2(o0, o1), SENT - 1u);
      st_dev(ws, oaddr, (v4u){ob, dppu<0x55>(ob), dppu<0xAA>(ob), dppu<0xFF>(ob)});
    }
    __builtin_amdgcn_sched_barrier(0);
    *(LAS u32*)(lds + L_FQ + 32 + sw * 4) = 1u;
    STAMP(6);
    STAMP_OUT();
    return;
  }

  const rsrc_t rk = mkrsrc(a.kc + segb, 1u << 20);
  v4u K[16], qg[4];
#pragma unroll
  for (int j = 0; j < 16; ++j) { K[j] = ld_nt(rk, kvo, ((j + rj) & 15) * 1024); LOADGAP(); }
  __builtin_amdgcn_sched_barrier(0);
  STAMP(11);
  __builtin_amdgcn_s_barrier();
  __builtin_amdgcn_s_setprio(2);
  LAS char* stg = lds + L_STG + sw * 16384;
  const u32 qo = WS_QG + (kvh * 2 + b) * 16384 + sw * 1024 + lane * 16;
#pragma unroll
  for (int j = 0; j <= 16; ++j) {
    if (j == QPOS) {
      __builtin_amdgcn_sched_barrier(0);
#pragma unroll
      for (int k = 0; k < 4; ++k) qg[k] = ld_dev(ws, qo + k * 4096);
      __builtin_amdgcn_sched_barrier(0);
    }
    if (j < 16) *(LAS v4u*)(stg + j * 1024 + g * 256 + ((c ^ (4 * g + (j & 3))) * 16)) = K[j];
  }
  asm volatile("" :: "v"(cpf));
  STAMP(1);
  v4u kf[4][4];
#pragma unroll
  for (int t = 0; t < 4; ++t)
#pragma unroll
    for (int kk = 0; kk < 4; ++kk) kf[t][kk] = *(LAS v4u*)(stg + (4 * t + (c & 3)) * 1024 + (c >> 2) * 256 + (((4 * kk + g) ^ c) * 16));
  const u64 tag = (((u64)__builtin_amdgcn_readfirstlane(ep.y) << 32) | (u64)(u32)__builtin_amdgcn_readfirstlane(ep.x)) + 1ull;
  const u32 thi = (u32)(tag >> 8), tlo = (u32)tag << 24;
  for (u32 sp = 0;; ++sp) {
    u32 bh = 0, bl = 0;
#pragma unroll
    for (int k = 0; k < 4; ++k) { bh |= (qg[k].y ^ thi) | (qg[k].w ^ thi); bl |= (qg[k].x ^ tlo) | (qg[k].z ^ tlo); }
    if (!__any((bh | (bl >> 24)) != 0u) || sp >= SPIN_CAP) break;
    asm volatile("" ::: "memory");
#pragma unroll
    for (int k = 0; k < 4; ++k) qg[k] = ld_dev(ws, qo + k * 4096);
  }
  { const float c0 = (asf(qg[0].x << 8) + asf(qg[1].x << 8)) + (asf(qg[2].x << 8) + asf(qg[3].x << 8));
    const float c1 = (asf(qg[0].z << 8) + asf(qg[1].z << 8)) + (asf(qg[2].z << 8) + asf(qg[3].z << 8));
    const u32 qq = pk2(c0, c1);
    LAS unsigned short* qp = (LAS unsigned short*)(lds + L_Q + sw * 256 + g * 64 + c * 2);
    qp[0] = (unsigned short)qq; qp[16] = (unsigned short)(qq >> 16);
    asm volatile("s_waitcnt lgkmcnt(0)" ::: "memory");
    *(LAS u32*)(lds + L_FQ + sw * 4) = 1u;
    STAMP(2);
    u32 sp = 0; v4u f;
    do { asm volatile("" ::: "memory"); f = *(LAS v4u*)(lds + L_FQ); } while (max4(f) == SENT && ++sp < SPIN_CAP);
    asm volatile("" ::: "memory"); }
  STAMP(3);
  v4f sc[4] = {Z4, Z4, Z4, Z4};
#pragma unroll
  for (int kk = 0; kk < 4; ++kk) {
    const v4u qf = *(LAS v4u*)(lds + L_Q + (c >> 2) * 256 + (4 * kk + g) * 16);
#pragma unroll
    for (int t = 0; t < 4; ++t) sc[t] = mfma(kf[t][kk], qf, sc[t]);
  }
  { float p[4];
    const bool u1 = c & 1, u2 = c & 2;
#pragma unroll
    for (int r = 0; r < 4; ++r) { const float lo = u1 ? sc[1][r] : sc[0][r], hi = u1 ? sc[3][r] : sc[2][r]; p[r] = u2 ? hi : lo; }
#pragma unroll
    for (int r = 0; r < 4; r += 2) {
      const u32 t = pk2(p[r], p[r + 1]);
      const u32 u = pk2(bflo(t) * 0.08838834764831845f, bfhi(t) * 0.08838834764831845f);
      p[r] = bflo(u); p[r + 1] = bfhi(u);
    }
    float mx = fmx(fmx3(p[0], p[1], p[2]), p[3]);
    mx = fmaxf(mx, dppf<XOR1>(mx)); mx = fmaxf(mx, dppf<XOR2>(mx));
    mx = max32(max16(mx));
    float ls = 0.f;
#pragma unroll
    for (int r = 0; r < 4; ++r) { p[r] = __builtin_amdgcn_exp2f((p[r] - mx) * 1.4426950408889634f); ls += p[r]; }
    ls += dppf<XOR1>(ls); ls += dppf<XOR2>(ls);
    ls = fold16(ls, ls); ls = fold32(ls, ls);
    const float rls = __builtin_amdgcn_rcpf(ls);
    *(LAS v2u*)(lds + L_P + sw * 512 + lane * 8) = (v2u){min(pk2(p[0] * rls, p[1] * rls), SENT - 1u), min(pk2(p[2] * rls, p[3] * rls), SENT - 1u)};
    if (lane < 16) *(LAS u32*)(lds + L_LW + (c >> 2) * 16 + sw * 4) = min(asu(fmaf(mx, 1.4426950408889634f, __builtin_amdgcn_logf(ls))), SENT - 1u); }
  if (sw == 0) {
    v4u l4; u32 sp = 0;
    do { asm volatile("" ::: "memory"); l4 = *(LAS v4u*)(lds + L_LW + (lane & 3) * 16); } while (__any(max4(l4) == SENT) && ++sp < SPIN_CAP);
    const float m = fmx(fmx3(asf(l4.x), asf(l4.y), asf(l4.z)), asf(l4.w));
    const float sm = (__builtin_amdgcn_exp2f(asf(l4.x) - m) + __builtin_amdgcn_exp2f(asf(l4.y) - m)) + (__builtin_amdgcn_exp2f(asf(l4.z) - m) + __builtin_amdgcn_exp2f(asf(l4.w) - m));
    const u32 lb = min(asu(m + __builtin_amdgcn_logf(sm)), SENT - 1u);
    st_dev(ws, lane == 0 ? box + 1024 : OOB, (v4u){lb, dppu<0x55>(lb), dppu<0xAA>(lb), dppu<0xFF>(lb)});
  }
  STAMP(4);
  __builtin_amdgcn_s_setprio(0);
  auto wait_flags = [&](int off) { u32 sp = 0; v4u f;
#pragma unroll 1
    do { asm volatile("" ::: "memory"); f = *(LAS v4u*)(lds + L_FQ + off); } while (max4(f) == SENT && ++sp < SPIN_CAP);
    asm volatile("" ::: "memory"); };

  if (role == 0) {
    { const rsrc_t r = mkrsrc(a.wo, 4u << 20);
      const u32 vo = (lane >> 2) * 1024 + (lane & 3) * 16, so = (kvh * 512 + sw * 128) * 1024 + i * 64;
      wait_flags(16);
#pragma unroll
      for (int k = 0; k < 4; ++k) { const int j = k + (i & 1) * 4; dma16<WAUX>(r, stg + j * 1024, vo, so + j * 16384); }
      wait_flags(32);
#pragma unroll
      for (int k = 0; k < 4; ++k) { const int j = k + 4 - (i & 1) * 4; dma16<WAUX>(r, stg + j * 1024, vo, so + j * 16384); } }
    STAMP(5);
    v4f wt[2];
    { u32 lw, sp = 0;
      do { asm volatile("" ::: "memory"); lw = __builtin_amdgcn_raw_buffer_load_b32(ws, WS_OB + (kvh * 32 + (lane & 31)) * BOX + 1024 + sw * 4, 0, 16); } while (lw == SENT && ++sp < SPIN_CAP);
      float m = asf(lw);
      m = fmaxf(m, dppf<XOR1>(m)); m = fmaxf(m, dppf<XOR2>(m)); m = fmaxf(m, dppf<HMIR>(m)); m = fmaxf(m, dppf<ROR8>(m));
      const float e = __builtin_amdgcn_exp2f(asf(lw) - m);
      *(LAS float*)(lds + L_WQ + 4096 + sw * 256 + lane * 4) = e * __builtin_amdgcn_rcpf(row_sum(e));
      wt[0] = *(LAS v4f*)(lds + L_WQ + 4096 + sw * 256 + g * 16); wt[1] = *(LAS v4f*)(lds + L_WQ + 4096 + sw * 256 + 64 + g * 16); }
    asm volatile("s_waitcnt vmcnt(0)" ::: "memory");
    STAMP(7);
    v4u bf[4][2];
#pragma unroll
    for (int ks = 0; ks < 4; ++ks)
#pragma unroll
      for (int nt = 0; nt < 2; ++nt) bf[ks][nt] = bfrag<64>(stg + ks * 2048 + nt * 32, lane);
    STAMP(8);
    v4u af[8];
    { u32 off[8];
#pragma unroll
      for (int k = 0; k < 8; ++k) off[k] = WS_OB + (kvh * 32 + c) * BOX + sw * 256 + g * 16 + (k >> 2) * (16 * BOX) + (k & 3) * 64;
      gather<8>(ws, off, af); }
    STAMP(9);
    float val[2][2];
#pragma unroll
    for (int r = 0; r < 2; ++r)
#pragma unroll
      for (int nt = 0; nt < 2; ++nt) {
        v4f d = Z4;
#pragma unroll
        for (int ks = 0; ks < 4; ++ks) d = mfma(af[4 * r + ks], bf[ks][nt], d);
        val[r][nt] = fmaf(d.x, wt[r][0], d.y * wt[r][1]) + fmaf(d.z, wt[r][2], d.w * wt[r][3]);
      }
    const float y = fold16(fold32(val[0][0], val[1][0]), fold32(val[0][1], val[1][1]));
    *(LAS float*)(lds + L_WQ + sw * 256 + lane * 4) = y;
    BARRIER();
    if (sw == 0) {
      v2f t[4];
#pragma unroll
      for (int u = 0; u < 4; ++u) t[u] = *(LAS v2f*)(lds + L_WQ + u * 256 + (lane & 31) * 8);
      const v2f o2 = (t[0] + t[1]) + (t[2] + t[3]);
      st_dev32(ws, lane < 32 ? WS_HB + (kvh * 16 + i) * 128 + lane * 4 : OOB, min(pk2(o2.x, o2.y), SENT - 1u));
    }
    STAMP(10);
    STAMP_OUT();
    return;
  }

  v4u xr[4];
  if (role < 3) {
#pragma unroll
    for (int ks = 0; ks < 4; ++ks) xr[ks] = *(const v4u*)(a.x + (role == 1 ? (c >> 3) * 1024 + sw * 256 + ks * 64 + g * 16 : (sw & 1) * 1024 + lane * 16));
    u32 bad, sp = 0;
    do {
      asm volatile("" ::: "memory");
      const v2u r = __builtin_amdgcn_raw_buffer_load_b64(ws, WS_QG + (sw * 2 + (lane >> 5)) * 32768 + (lane & 31) * 512, 0, 16);
      bad = (r.y ^ thi) | ((r.x ^ tlo) >> 24);
    } while (bad != 0u && ++sp < SPIN_CAP);
  }
  wait_flags(FFN_EARLY ? 16 : 32);
  if (FFN_DELAY) __builtin_amdgcn_s_sleep(FFN_DELAY);
  if (role == 3) { STAMP_OUT(); return; }
  if (role == 1) {
    const u32 vo = (lane >> 2) * 3584 + (lane & 3) * 16, so = sw * 128 * 3584 + gidx * 64;
    const int m0 = gidx & 1;
    const rsrc_t r0 = mkrsrc(m0 ? a.wu : a.wg, 512 * 3584), r1 = mkrsrc(m0 ? a.wg : a.wu, 512 * 3584);
#pragma unroll
    for (int j = 0; j < 8; ++j) dma16<WAUX>(r0, stg + m0 * 8192 + j * 1024, vo, so + j * 57344);
#pragma unroll
    for (int j = 0; j < 8; ++j) dma16<WAUX>(r1, stg + (m0 ^ 1) * 8192 + j * 1024, vo, so + j * 57344);
  } else {
    const rsrc_t r = mkrsrc(a.wd, 1792 * 1024);
    const u32 vo = (lane >> 1) * 1024 + (lane & 1) * 16, so = sw * 14 * 32768 + didx * 32;
    int j = ((i - 23) * 7) >> 1;
#pragma unroll
    for (int k = 0; k < 14; ++k) { dma16<WAUX>(r, stg + j * 1024, vo, so + j * 32768); j = j == 13 ? 0 : j + 1; }
  }
  STAMP(5);

  if (role == 1) {
    v4u xa[4];
#pragma unroll
    for (int ks = 0; ks < 4; ++ks) xa[ks] = (c & 7) ? (v4u){0u, 0u, 0u, 0u} : xr[ks];
    asm volatile("s_waitcnt vmcnt(0)" ::: "memory");
    STAMP(7);
    v4u bf[4][4];
#pragma unroll
    for (int ks = 0; ks < 4; ++ks)
#pragma unroll
      for (int q = 0; q < 4; ++q) bf[ks][q] = bfrag<64>(stg + (q >> 1) * 8192 + ks * 2048 + (q & 1) * 32, lane);
    const v4u ones = {0x3f803f80u, 0x3f803f80u, 0x3f803f80u, 0x3f803f80u};
    v4f ac[5] = {Z4, Z4, Z4, Z4, Z4}, cs[4] = {Z4, Z4, Z4, Z4};
#pragma unroll
    for (int ks = 0; ks < 4; ++ks) {
#pragma unroll
      for (int q = 0; q < 4; ++q) { ac[q] = mfma(xa[ks], bf[ks][q], ac[q]); cs[q] = mfma(ones, bf[ks][q], cs[q]); }
      ac[4] = mfma(xa[ks], ones, ac[4]);
    }
    *(LAS v2f*)(lds + L_WQ + 8192 + sw * 512 + lane * 8) = (g & 1) ? (v2f){cs[1].x, cs[3].x} : (v2f){cs[0].x, cs[2].x};
    BARRIER();
    v2f csum;
    { v2f t[4];
#pragma unroll
      for (int u = 0; u < 4; ++u) t[u] = *(LAS v2f*)(lds + L_WQ + 8192 + u * 512 + lane * 8);
      csum = (t[0] + t[1]) + (t[2] + t[3]); }
    STAMP(8);
    v4u af[4];
    { const u32 o0 = WS_HB + ((c & 7) * 16 + sw * 4) * 128 + (c >> 3) * 64 + g * 16; const u32 off[4] = {o0, o0 + 128, o0 + 256, o0 + 384}; gather<4>(ws, off, af); }
    STAMP(9);
    float r5[5];
#pragma unroll
    for (int q = 0; q < 5; ++q) {
      v4f d = ac[q];
#pragma unroll
      for (int ks = 0; ks < 4; ++ks) d = mfma(af[ks], q < 4 ? bf[ks][q] : ones, d);
      r5[q] = (d.x + d.y) + (d.z + d.w);
    }
    *(LAS v4f*)(lds + L_WQ + sw * 1024 + lane * 16) = (v4f){fold16(r5[0], r5[1]), fold16(r5[2], r5[3]), fold16(r5[4], r5[4]), 0.f};
    BARRIER();
    if (sw == 0) {
      v4f t[4];
#pragma unroll
      for (int u = 0; u < 4; ++u) t[u] = *(LAS v4f*)(lds + L_WQ + u * 1024 + lane * 16);
      const v4f z = (t[0] + t[1]) + (t[2] + t[3]);
      const float mb = bfr(z.z * (1.f / 512.f));
      const float hid = fmaf(-mb, csum.x, z.x) * fmaf(-mb, csum.y, z.y);
      const u32 hb = min(pk2(hid, dppf<XOR1>(hid)), SENT - 1u);
      st_dev32(ws, (lane & 1) == 0 ? WS_HID + gidx * 128 + lane * 2 : OOB, hb);
    }
    STAMP(10);
    STAMP_OUT();
    return;
  }

  {
    asm volatile("s_waitcnt vmcnt(0)" ::: "memory");
    __builtin_amdgcn_s_barrier();
    asm volatile("" ::: "memory");
    STAMP(7);
    v4u bf[14];
#pragma unroll
    for (int kb = 0; kb < 14; ++kb) bf[kb] = bfrag<32>(stg + kb * 1024, lane);
    if (sw < 2) {
      v4u hp[8];
      { u32 off[8];
#pragma unroll
        for (int u = 0; u < 8; ++u) off[u] = WS_HB + u * 2048 + (lane >> 2) * 128 + sw * 64 + (lane & 3) * 16;
        gather<8>(ws, off, hp); }
      float h[8], sm = 0.f;
#pragma unroll
      for (int d = 0; d < 4; ++d) {
        float lo = 0.f, hi = 0.f;
#pragma unroll
        for (int u = 0; u < 8; ++u) { lo += bflo(hp[u][d]); hi += bfhi(hp[u][d]); }
        const u32 t = pk2(lo, hi), hh = pk2(bflo(xr[0][d]) + bflo(t), bfhi(xr[0][d]) + bfhi(t));
        h[2 * d] = bflo(hh); h[2 * d + 1] = bfhi(hh);
        sm += h[2 * d] + h[2 * d + 1];
      }
      const float mean = wave_sum(sm) * (1.f / 512.f);
      float sq = 0.f;
#pragma unroll
      for (int e = 0; e < 8; ++e) { const float t = h[e] - mean; sq = fmaf(t, t, sq); }
      const float inv = __builtin_amdgcn_rcpf(ln_sd(wave_sum(sq) * (1.f / 512.f)));
      if ((lane >> 1) == didx) {
        *(LAS v4f*)(lds + L_H + sw * 64 + (lane & 1) * 32) = (v4f){h[0], h[1], h[2], h[3]};
        *(LAS v4f*)(lds + L_H + sw * 64 + (lane & 1) * 32 + 16) = (v4f){h[4], h[5], h[6], h[7]};
      }
      if (lane == 0) *(LAS float*)(lds + L_H + 128 + sw * 4) = inv * inv;
    }
    STAMP(8);
    v4u af[2];
    { const u32 o0 = (c & 7) < 7 ? WS_HID + (sw * 14 + (c & 7)) * 128 + (c >> 3) * 64 + g * 16 : OOB; const u32 off[2] = {o0, o0 + 896}; gather<2>(ws, off, af); }
    STAMP(9);
    v4f d[7];
#pragma unroll
    for (int kb = 0; kb < 7; ++kb) d[kb] = mfma(af[1], bf[7 + kb], mfma(af[0], bf[kb], Z4));
    const bool odd = g & 1;
    float y = ((odd ? d[4].x : d[0].x) + (odd ? d[5].y : d[1].y)) + ((odd ? d[6].z : d[2].z) + (odd ? 0.f : d[3].w));
    y = fold16(y, y);
    *(LAS float*)(lds + L_WQ + sw * 256 + lane * 4) = y;
    BARRIER();
    if (sw == 0) {
      if (lane < 16) {
        const int r = lane >> 3, cp = lane & 7;
        v2f t[4];
#pragma unroll
        for (int u = 0; u < 4; ++u) t[u] = *(LAS v2f*)(lds + L_WQ + u * 256 + r * 128 + cp * 8);
        const v2f z = (t[0] + t[1]) + (t[2] + t[3]);
        const v2f hh = *(LAS v2f*)(lds + L_H + r * 64 + cp * 8);
        const float sc2 = *(LAS float*)(lds + L_H + 128 + r * 4);
        const u32 f2 = pk2(z.x * sc2, z.y * sc2);
        *(u32*)(a.out + r * 1024 + didx * 32 + cp * 4) = pk2(hh.x + bflo(f2), hh.y + bfhi(f2));
      }
      st_dev64(ws, (w == 23 * 8 && lane == 0) ? WS_EPOCH : OOB, (v2u){(u32)tag, (u32)(tag >> 32)});
    }
    STAMP(10);
    STAMP_OUT();
  }
}

extern "C" void kernel_launch(void* const* d_in, const int* in_sizes, int n_in, void* d_out, int out_size, void* d_ws, size_t ws_size,
                              hipStream_t stream) {
  Args a;
  a.x = (const char*)d_in[0]; a.kc = (const char*)d_in[1]; a.vc = (const char*)d_in[2]; a.wq = (const char*)d_in[3]; a.wo = (const char*)d_in[4];
  a.wg = (const char*)d_in[6]; a.wu = (const char*)d_in[7]; a.wd = (const char*)d_in[8];
  a.out = (char*)d_out; a.ws = (char*)d_ws;
  fused_decode<<<256, 512, 0, stream>>>(a);
}
```

```cpp
#define VWIN 4
#define TOUCH 2
#define TOUCHD 2
#define CODE_PF 119
#include <hip/hip_runtime.h>
#include <stdint.h>

typedef unsigned int u32;
typedef unsigned long long u64;
typedef u32 v4u __attribute__((ext_vector_type(4)));
typedef u32 v2u __attribute__((ext_vector_type(2)));
typedef float v4f __attribute__((ext_vector_type(4)));
typedef float v2f __attribute__((ext_vector_type(2)));
typedef short v8s __attribute__((ext_vector_type(8)));
typedef short v4s __attribute__((ext_vector_type(4)));
typedef __bf16 v2b __attribute__((ext_vector_type(2)));
typedef __amdgpu_buffer_rsrc_t rsrc_t;
#define LAS __attribute__((address_space(3)))
#define GAS __attribute__((address_space(1)))

#define QPOS 11
#define KWIN 5
#define EARLY1 6
#define EARLY2 8
#define FFN_AT 2
#define FFN_DELAY 0
#define DN_DELAY 0
#define DBG 0
#define TW 0
#define TG 0
#define TD 0
#define WO_DELAY 0
#define WWIN 0
#define WPOS -1
#define W2AT 48
#define FAST 0
#define VSLOW 7
#define WAUX 0
#define KVAUX 2
#define EXW 184
#define GRPSTRIDE (262144 + 28672)
#define KVMAP 1

constexpr int SK = 32768, WIN = 4096;
constexpr u32 SENT = 0xFFFFFFFFu, OOB = 0x80000000u;
constexpr u32 SPIN_CAP = 1u << 18;
constexpr u32 WS_EPOCH = 0;
constexpr u32 WS_HB = 4096;
constexpr u32 WS_HID = 20480;
constexpr u32 WS_QG = 32768, WS_OB = WS_QG + 8192, BOX = 1152, GRP = GRPSTRIDE;
constexpr u32 WS_BYTES = WS_QG + 16 * GRP;
constexpr int L_ORED = 0;
constexpr int L_P = 8192;
constexpr int L_QP = 10240;
constexpr int L_LW = 12288;
constexpr int L_FQ = 12352;
constexpr int L_INIT = 12416;
constexpr int L_Q = 12416;
constexpr int L_H = 13440;
constexpr int L_TAG = 13600;
constexpr int L_WQ = 13824;
constexpr int L_STG = 30208;
constexpr int L_WTS = 95744;
constexpr int L_BYTES = L_WTS + 65536 + 128;

struct Args {
  const char *x, *kc, *vc, *wq, *wo, *wg, *wu, *wd;
  char *out, *ws;
};

#define NWAVES 8
#define STAMP(i) do {} while (0)
#define STAMP_OUT() do {} while (0)

__device__ __forceinline__ rsrc_t mkrsrc(const void* p, u32 bytes) { return __builtin_amdgcn_make_buffer_rsrc((void*)p, 0, bytes, 0x00020000); }
__device__ __forceinline__ v4u ld_nt(rsrc_t r, u32 voff, u32 soff) { return __builtin_amdgcn_raw_buffer_load_b128(r, voff, soff, KVAUX); }
__device__ __forceinline__ v4u ld_dev(rsrc_t r, u32 off) { return __builtin_amdgcn_raw_buffer_load_b128(r, off, 0, 16); }
__device__ __forceinline__ void st_dev(rsrc_t r, u32 off, v4u v) { __builtin_amdgcn_raw_buffer_store_b128(v, r, off, 0, 16); }
__device__ __forceinline__ void st_dev32(rsrc_t r, u32 off, u32 v) { __builtin_amdgcn_raw_buffer_store_b32(v, r, off, 0, 16); }
__device__ __forceinline__ void st_dev64(rsrc_t r, u32 off, v2u v) { __builtin_amdgcn_raw_buffer_store_b64(v, r, off, 0, 16); }
template <int AUX = 2> __device__ __forceinline__ void dma16(rsrc_t r, LAS char* dst, u32 voff, u32 soff) { __builtin_amdgcn_raw_ptr_buffer_load_lds(r, (LAS void*)dst, 16, voff, soff, 0, AUX); }
__device__ __forceinline__ u32 touch(const char* p) { return WAUX ? __hip_atomic_load((const GAS u32*)p, __ATOMIC_RELAXED, __HIP_MEMORY_SCOPE_AGENT) : *(const u32*)p; }
__device__ __forceinline__ float asf(u32 x) { return __builtin_bit_cast(float, x); }
__device__ __forceinline__ u32 asu(float x) { return __builtin_bit_cast(u32, x); }
__device__ __forceinline__ u32 pk2(float lo, float hi) { v2f f = {lo, hi}; return __builtin_bit_cast(u32, __builtin_convertvector(f, v2b)); }
__device__ __forceinline__ float bflo(u32 x) { return asf(x << 16); }
__device__ __forceinline__ float bfhi(u32 x) { return asf(x & 0xffff0000u); }
__device__ __forceinline__ float bfr(float x) { return bflo(pk2(x, x)); }
template <int CTRL> __device__ __forceinline__ float dppf(float x) { return asf((u32)__builtin_amdgcn_mov_dpp((int)asu(x), CTRL, 0xf, 0xf, true)); }
template <int CTRL> __device__ __forceinline__ u32 dppu(u32 x) { return (u32)__builtin_amdgcn_mov_dpp((int)x, CTRL, 0xf, 0xf, true); }
constexpr int XOR1 = 0xB1, XOR2 = 0x4E, HMIR = 0x141, ROR8 = 0x128;
__device__ __forceinline__ float fmx(float a, float b) { float r; asm("v_max_f32 %0, %1, %2" : "=v"(r) : "v"(a), "v"(b)); return r; }
__device__ __forceinline__ float fmx3(float a, float b, float c) { float r; asm("v_max3_f32 %0, %1, %2, %3" : "=v"(r) : "v"(a), "v"(b), "v"(c)); return r; }
__device__ __forceinline__ float fold32(float a, float b) { auto s = __builtin_amdgcn_permlane32_swap(asu(a), asu(b), false, false); return asf(s[0]) + asf(s[1]); }
__device__ __forceinline__ float fold16(float a, float b) { auto s = __builtin_amdgcn_permlane16_swap(asu(a), asu(b), false, false); return asf(s[0]) + asf(s[1]); }
__device__ __forceinline__ float max32(float a) { auto s = __builtin_amdgcn_permlane32_swap(asu(a), asu(a), false, false); return fmaxf(asf(s[0]), asf(s[1])); }
__device__ __forceinline__ float max16(float a) { auto s = __builtin_amdgcn_permlane16_swap(asu(a), asu(a), false, false); return fmaxf(asf(s[0]), asf(s[1])); }
__device__ __forceinline__ float row_sum(float x) { x += dppf<XOR1>(x); x += dppf<XOR2>(x); x += dppf<HMIR>(x); x += dppf<ROR8>(x); return x; }
__device__ __forceinline__ float wave_sum(float x) { x = row_sum(x); x = fold16(x, x); return fold32(x, x); }
__device__ __forceinline__ v4f mfma(v4u a, v4u b, v4f c) { return __builtin_amdgcn_mfma_f32_16x16x32_bf16(__builtin_bit_cast(v8s, a), __builtin_bit_cast(v8s, b), c, 0, 0, 0); }
__device__ __forceinline__ u32 max4(v4u v) { return max(max(v.x, v.y), max(v.z, v.w)); }
__device__ __forceinline__ v2u tr_read(LAS char* p) { return __builtin_bit_cast(v2u, __builtin_amdgcn_ds_read_tr16_b64_v4i16((LAS v4s*)p)); }
__device__ __forceinline__ v4u bfrag_at(LAS char* p, int hi_off) { v2u lo = tr_read(p), hi = tr_read(p + hi_off); return (v4u){lo.x, lo.y, hi.x, hi.y}; }
#define BARRIER() do { asm volatile("s_waitcnt lgkmcnt(0)" ::: "memory"); __builtin_amdgcn_s_barrier(); asm volatile("" ::: "memory"); } while (0)
#define SCHED() __builtin_amdgcn_sched_barrier(0)
#define PIN(x) asm volatile("" : "+v"(x))
#define Z4 ((v4f){0.f, 0.f, 0.f, 0.f})
__device__ __forceinline__ float ln_sd(float var) { return bfr(__builtin_amdgcn_sqrtf(bfr(bfr(var) + 1e-5f))); }
__device__ __forceinline__ u32 umax3(u32 a, u32 b, u32 c) { return max(max(a, b), c); }
__device__ __forceinline__ float hsum(v4f d) { const v2f t = d.xy + d.zw; return t.x + t.y; }

template <int N, int STEP> __device__ __forceinline__ void request(rsrc_t r, u32 base, v4u (&v)[N]) {
#pragma unroll
  for (int k = 0; k < N; ++k) v[k] = __builtin_amdgcn_raw_buffer_load_b128(r, base + (STEP * k < 4096 ? STEP * k : 0), STEP * k < 4096 ? 0 : STEP * k, 16);
}
template <int N> __device__ __forceinline__ bool incomplete(const v4u (&v)[N]) {
  u32 m = 0;
#pragma unroll
  for (int k = 0; k < N; ++k) m = umax3(umax3(m, v[k].x, v[k].y), v[k].z, v[k].w);
  return m == SENT;
}
template <int N, int STEP> __device__ __forceinline__ void gather(rsrc_t r, u32 base, v4u (&v)[N]) {
  u32 m, sp = 0;
  do {
    asm volatile("" ::: "memory");
#pragma unroll
    for (int k = 0; k < N; ++k) v[k] = __builtin_amdgcn_raw_buffer_load_b128(r, base + (STEP * k < 4096 ? STEP * k : 0), STEP * k < 4096 ? 0 : STEP * k, 16);
    m = 0;
#pragma unroll
    for (int k = 0; k < N; ++k) m = umax3(umax3(m, v[k].x, v[k].y), v[k].z, v[k].w);
  } while (m == SENT && ++sp < SPIN_CAP);
}

__global__ void __launch_bounds__(512, 2) fused_decode(Args a) {
  __shared__ __attribute__((aligned(16))) char smem[L_BYTES];
  LAS char* lds = (LAS char*)smem;
  const int w = blockIdx.x, lane = threadIdx.x & 63, wave = __builtin_amdgcn_readfirstlane(threadIdx.x >> 6);
  STAMP(0);
  const int kvh = w & 7, i = w >> 3, b = i >> 4, s = i & 15, g = lane >> 4, c = lane & 15, sw = wave & 3;
  u32 cpf;
  { u64 pc; asm volatile("s_getpc_b64 %0" : "=s"(pc));
    cpf = *(const GAS u32*)(pc + min((s * 8 + wave), CODE_PF - 1) * 128); }
  { const v4u S4 = {SENT, SENT, SENT, SENT};
    *(LAS v4u*)(lds + threadIdx.x * 16) = S4;
    *(LAS v4u*)(lds + L_INIT - 8192 + threadIdx.x * 16) = S4; }
  const int role = i < 16 ? 0 : i < 23 ? 1 : i < 27 ? 2 : 3;
  const int gidx = kvh * 7 + (i - 16), didx = kvh * 4 + (i - 23);
  const u32 box = WS_OB + (kvh * 2 + b) * GRP + s * BOX;
  const u32 rline = role == 0 ? WS_HB + (kvh * 16 + i) * 128 : WS_HID + gidx * 128;
  const int cgq = i & 7, kq = i >> 3;
  const bool slow = !((FAST >> role) & 1);
  u32 xo0 = lane * 32, xo1 = ((c >> 2) & 1) * 1024 + kq * 256 + sw * 64 + g * 16;
  u32 wqv = (lane >> 3) * 8192 + (lane & 7) * 16;
  u32 clro = lane < 16 ? box + sw * 256 + lane * 16 : (sw == 0 ? box + 1024 - 256 : rline - 256) + lane * 16;
  if (lane >= (sw == 0 ? 17 : (sw == 1 && role < 2) ? 24 : 16)) clro = OOB;
  u32 kvo = (KVMAP ? s * 16384 + ((sw + i + (i >> 2)) & 3) * 262144 : s * 65536 + ((sw + i + (i >> 2)) & 3) * 16384) + lane * 16;
  u32 wqs = (kq * 128 + sw * 32) * 8192 + kvh * 1024 + cgq * 128, rj = (sw * 5 + i * 3 + kvh * 2) * 1024, seg = (b * 8 + kvh) * 8 + 7;
  PIN(xo0); PIN(xo1); PIN(wqv); PIN(clro); PIN(kvo);
  asm volatile("" : "+s"(wqs), "+s"(rj), "+s"(seg));
  SCHED();
  const rsrc_t ws = mkrsrc(a.ws, WS_BYTES);
  v4u x0, x1, xf; v2u ep;
  if (wave < 4) {
    const rsrc_t rx = mkrsrc(a.x, 2048), rq = mkrsrc(a.wq, 4u << 20);
    x0 = __builtin_amdgcn_raw_buffer_load_b128(rx, xo0, 0, 0); x1 = __builtin_amdgcn_raw_buffer_load_b128(rx, xo0 + 16, 0, 0);
    xf = __builtin_amdgcn_raw_buffer_load_b128(rx, xo1, 0, 0);
    SCHED();
#pragma unroll
    for (int j = 0; j < 4; ++j) dma16<KVAUX>(rq, lds + L_WQ + sw * 4096 + j * 1024, wqv, wqs + j * 65536);
    st_dev(ws, clro, (v4u){SENT, SENT, SENT, SENT});
    ep = __builtin_amdgcn_raw_buffer_load_b64(ws, sw == 0 ? WS_EPOCH : OOB, 0, 16);
  }
  SCHED();
  asm volatile("s_waitcnt lgkmcnt(0)\n\ts_barrier" ::: "memory");
  SCHED();
  STAMP(12);
  int wave2 = wave;
  asm volatile("" : "+s"(wave2));

  if (wave2 < 4) {
    v4u xa;
    { v2f f[8];
#pragma unroll
      for (int d = 0; d < 4; ++d) { f[d] = (v2f){bflo(x0[d]), bfhi(x0[d])}; f[4 + d] = (v2f){bflo(x1[d]), bfhi(x1[d])}; }
      const v2f s2 = ((f[0] + f[1]) + (f[2] + f[3])) + ((f[4] + f[5]) + (f[6] + f[7]));
      float sm = row_sum(s2.x + s2.y);
      const float mean = fold16(sm, sm) * (1.f / 512.f);
      v2f q2 = {0.f, 0.f};
#pragma unroll
      for (int e = 0; e < 8; ++e) { const v2f t = f[e] - mean; q2 = t * t + q2; }
      float sq = row_sum(q2.x + q2.y);
      const float inv0 = __builtin_amdgcn_rcpf(ln_sd(fold16(sq, sq) * (1.f / 512.f))), mb0 = bfr(mean);
      const bool r1 = c & 4;
      const float mb = r1 ? asf(__builtin_amdgcn_readlane(asu(mb0), 32)) : asf(__builtin_amdgcn_readlane(asu(mb0), 0));
      const float inv = r1 ? asf(__builtin_amdgcn_readlane(asu(inv0), 32)) : asf(__builtin_amdgcn_readlane(asu(inv0), 0));
#pragma unroll
      for (int d = 0; d < 4; ++d) { const u32 t = pk2(bflo(xf[d]) - mb, bfhi(xf[d]) - mb); xa[d] = pk2(bflo(t) * inv, bfhi(t) * inv); }
    }
    PIN(xa);
    STAMP(11);
    asm volatile("s_waitcnt vmcnt(0)" ::: "memory");
    asm volatile("" :: "v"(cpf));
    STAMP(1);
    { v4u qp; LAS char* wp = lds + L_WQ + sw * 4096 + (g * 8 + (c >> 2)) * 128 + (c & 3) * 8;
#pragma unroll
      for (int nt = 0; nt < 4; ++nt) qp[nt] = min(asu(mfma(xa, bfrag_at(wp + nt * 32, 512), Z4).x), SENT - 1u);
      *(LAS v4u*)(lds + L_QP + sw * 512 + (lane & 31) * 16) = qp; }
    if (sw == 0) {
      v4u t[4]; u32 sp = 0, m;
      do {
        asm volatile("" ::: "memory");
        m = 0;
#pragma unroll
        for (int u = 0; u < 4; ++u) { t[u] = *(LAS v4u*)(lds + L_QP + u * 512 + (lane & 31) * 16); m = umax3(umax3(m, t[u].x, t[u].y), t[u].z, t[u].w); }
      } while (__any(m == SENT) && ++sp < SPIN_CAP);
      u32 q[4];
#pragma unroll
      for (int e = 0; e < 4; ++e) q[e] = (asu((asf(t[0][e]) + asf(t[1][e])) + (asf(t[2][e]) + asf(t[3][e]))) + 0x200u) >> 10;
      const u64 tag = (((u64)__builtin_amdgcn_readfirstlane(ep.y) << 32) | (u64)(u32)__builtin_amdgcn_readfirstlane(ep.x)) + 1ull;
      const u32 ta = (u32)tag << 12, tb = ((u32)(tag >> 20) ^ (u32)tag) << 12;
      st_dev(ws, lane < 32 ? WS_QG + (kvh * 2 + g) * GRP + kq * 2048 + cgq * 256 + c * 16 : OOB,
             (v4u){q[0] | (q[1] << 22), (q[1] >> 10) | ta, q[2] | (q[3] << 22), (q[3] >> 10) | tb});
      *(LAS v2u*)(lds + L_TAG) = (v2u){(u32)tag, (u32)(tag >> 32)};
    }
    u32 tch[2] = {0u, 0u};
    if (sw >= 2) { if (TW) tch[0] = touch(a.wo + (size_t)(kvh * 512 + i * 16 + (sw & 1) * 8) * 1024 + lane * 128); }
    else if (TG > sw) tch[0] = touch((sw ? a.wu : a.wg) + (size_t)(i * 16 + (lane >> 2)) * 3584 + ((kvh * 448) & ~127) + (lane & 3) * 128);
    if (TD && sw == 1) tch[1] = touch(a.wd + (size_t)(i * 56 + min(lane, 55)) * 1024 + kvh * 128);
    STAMP(2);
    BARRIER();
    SCHED();
    const rsrc_t rv = mkrsrc(a.vc + ((size_t)seg << 20), 1u << 20);
    v4u V[16], vt[2][8];
    const rsrc_t rwo = mkrsrc(a.wo, role == 0 ? 4u << 20 : 0);
    u32 wov = (lane >> 3) * 1024 + ((lane & 7) ^ ((lane >> 3) & 2)) * 16, wov1 = wov ^ 64;
    PIN(wov); PIN(wov1);
    auto wo_first = [&]() {
#pragma unroll
      for (int k = 0; k < 8; ++k) dma16<WAUX>(rwo, lds + (role == 0 ? L_WTS + sw * 16384 + (kq & 1) * 8192 : L_WQ) + k * 1024, (k & 1) ? wov1 : wov, (kvh * 512 + sw * 128 + (kq & 1) * 64 + k * 8) * 1024 + cgq * 128);
      SCHED(); };
    auto vperm = [&](int m) {
#pragma unroll
      for (int d = 0; d < 4; ++d) {
        u32 lo = __builtin_amdgcn_perm(V[2 * m + 1][d], V[2 * m][d], 0x05040100u), hi = __builtin_amdgcn_perm(V[2 * m + 1][d], V[2 * m][d], 0x07060302u);
        PIN(lo); PIN(hi);
        vt[m >> 2][2 * d][m & 3] = lo; vt[m >> 2][2 * d + 1][m & 3] = hi;
      }
      SCHED();
      if (m == EARLY1) *(LAS u32*)(lds + L_FQ + 16 + sw * 4) = 1u;
      if (m == WPOS) wo_first();
    };
#pragma unroll
    for (int m = 0; m < 8; ++m) {
      V[2 * m] = ld_nt(rv, kvo, (2 * m * 1024 + rj) & 15360); V[2 * m + 1] = ld_nt(rv, kvo, ((2 * m + 1) * 1024 + rj) & 15360);
      SCHED();
      if (m == 7 && WPOS < 0) wo_first();
      if (m >= VSLOW && m < 7 && slow) asm volatile("s_waitcnt vmcnt(%0)" :: "i"(2 * VSLOW) : "memory");
      SCHED();
      if (m >= VWIN) vperm(m - VWIN);
    }
    asm volatile("" :: "v"(tch[0]), "v"(tch[1]));
    __builtin_amdgcn_s_setprio(3);
    STAMP(3);
    v4u pf0, pf1; v4f cw;
    { v2u pp; v4u l4; u32 sp = 0;
#pragma unroll 1
      do { asm volatile("" ::: "memory"); pp = *(LAS v2u*)(lds + L_P + sw * 512 + lane * 8); l4 = *(LAS v4u*)(lds + L_LW + sw * 16);
           if (!__any(umax3(umax3(pp.x, pp.y, l4.x), umax3(l4.y, l4.z, l4.w), 0u) == SENT)) break;
           __builtin_amdgcn_s_sleep(1);
      } while (++sp < SPIN_CAP);
      pf0 = (v4u){dppu<0x00>(pp.x), dppu<0x00>(pp.y), dppu<0x55>(pp.x), dppu<0x55>(pp.y)};
      pf1 = (v4u){dppu<0xAA>(pp.x), dppu<0xAA>(pp.y), dppu<0xFF>(pp.x), dppu<0xFF>(pp.y)};
      const float m = fmx(fmx3(asf(l4.x), asf(l4.y), asf(l4.z)), asf(l4.w));
#pragma unroll
      for (int u = 0; u < 4; ++u) cw[u] = __builtin_amdgcn_exp2f(asf(l4[u]) - m);
      cw *= __builtin_amdgcn_rcpf(hsum(cw)); }
    LAS char* rp = lds + L_ORED + (lane & 3) * 512 + (sw * 16 + (lane >> 2)) * 8;
    LAS char* wp = lds + L_ORED + sw * 2048 + lane * 8; LAS char* wp1 = wp + 1024;
    u32 oaddr = (lane & 3) == 0 ? box + sw * 256 + lane * 4 : OOB;
    PIN(oaddr); PIN(rp); PIN(wp); PIN(wp1); PIN(cw); PIN(pf0); PIN(pf1);
    STAMP(4);
    v4f oa[8];
#pragma unroll
    for (int m = 8 - VWIN; m < 4; ++m) vperm(m);
#pragma unroll
    for (int e = 0; e < 8; ++e) oa[e] = mfma(pf0, vt[0][e], Z4);
    SCHED();
#pragma unroll
    for (int m = (8 - VWIN > 4 ? 8 - VWIN : 4); m < 7; ++m) vperm(m);
#pragma unroll
    for (int d = 0; d < 4; ++d) {
      vt[1][2 * d][3] = __builtin_amdgcn_perm(V[15][d], V[14][d], 0x05040100u); vt[1][2 * d + 1][3] = __builtin_amdgcn_perm(V[15][d], V[14][d], 0x07060302u);
      oa[2 * d] = mfma(pf1, vt[1][2 * d], oa[2 * d]); oa[2 * d + 1] = mfma(pf1, vt[1][2 * d + 1], oa[2 * d + 1]);
    }
    SCHED();
    STAMP(5);
#pragma unroll
    for (int d = 0; d < 4; ++d) { LAS char* q = (d < 2 ? wp : wp1) + (d & 1) * 512; *(LAS float*)q = oa[2 * d].x; *(LAS float*)(q + 4) = oa[2 * d + 1].x; }
    { v2u t[4]; u32 sp = 0, m;
      do {
        asm volatile("" ::: "memory");
#pragma unroll
        for (int u = 0; u < 4; ++u) t[u] = *(LAS v2u*)(rp + u * 2048);
        m = umax3(umax3(t[0].x, t[0].y, t[1].x), umax3(t[1].y, t[2].x, t[2].y), max(t[3].x, t[3].y));
      } while (__any(m == SENT) && ++sp < (1u << 14));
      const v2f o2 = (__builtin_bit_cast(v2f, t[0]) * cw.x + __builtin_bit_cast(v2f, t[1]) * cw.y) + (__builtin_bit_cast(v2f, t[2]) * cw.z + __builtin_bit_cast(v2f, t[3]) * cw.w);
      const u32 ob = min(pk2(o2.x, o2.y), SENT - 1u);
      st_dev(ws, oaddr, (v4u){ob, dppu<0x55>(ob), dppu<0xAA>(ob), dppu<0xFF>(ob)});
    }
    SCHED();
    if (EARLY1 >= 7) *(LAS u32*)(lds + L_FQ + 16 + sw * 4) = 1u;
    *(LAS u32*)(lds + L_FQ + 48 + sw * 4) = 1u;
    STAMP(6);
    asm volatile("s_waitcnt vmcnt(1)" ::: "memory");
    *(LAS u32*)(lds + L_FQ + 32 + sw * 4) = 1u;
    STAMP(7);
    STAMP_OUT();
    return;
  }

  const rsrc_t rk = mkrsrc(a.kc + ((size_t)seg << 20), 1u << 20);
  LAS char* stg = lds + L_STG + sw * 16384;
  const u32 qo = WS_QG + (kvh * 2 + b) * GRP + (lane >> 5) * 2048 + sw * 512 + (lane & 31) * 16;
  v4u qg[2];
  {
    const u32 kb = kvo - lane * 16 + g * 256;
#pragma unroll
    for (int j = 0; j < 16; ++j) {
      dma16<KVAUX>(rk, stg + j * 1024, kb + ((c ^ (4 * g + (j & 3))) * 16), (j * 1024 + rj) & 15360);
      SCHED();
      constexpr int JQ = QPOS + KWIN - 1;
      if (j >= KWIN && j < 15 && slow) { if (j > JQ && j <= JQ + KWIN) asm volatile("s_waitcnt vmcnt(%0)" :: "i"(KWIN + 2) : "memory"); else asm volatile("s_waitcnt vmcnt(%0)" :: "i"(KWIN) : "memory"); }
      if (j == JQ && j < 15) { qg[0] = ld_dev(ws, qo); qg[1] = __builtin_amdgcn_raw_buffer_load_b128(ws, qo, 4096, 16); }
      SCHED();
    }
    STAMP(11);
    __builtin_amdgcn_s_barrier();
    __builtin_amdgcn_s_setprio(2);
    if (QPOS + KWIN - 1 >= 15) {
      asm volatile("s_waitcnt vmcnt(%0)" :: "i"(16 - QPOS) : "memory");
      qg[0] = ld_dev(ws, qo); qg[1] = __builtin_amdgcn_raw_buffer_load_b128(ws, qo, 4096, 16);
      SCHED(); } }
  asm volatile("" :: "v"(cpf));
  STAMP(1);
  u64 tag;
  { const v2u t2 = *(LAS v2u*)(lds + L_TAG); tag = ((u64)__builtin_amdgcn_readfirstlane(t2.y) << 32) | (u64)(u32)__builtin_amdgcn_readfirstlane(t2.x); }
  const u32 ta = (u32)tag << 12, tb = ((u32)(tag >> 20) ^ (u32)tag) << 12;
  auto qbad = [&]() -> bool { return __any((((qg[0].y ^ ta) | (qg[0].w ^ tb) | (qg[1].y ^ ta) | (qg[1].w ^ tb)) >> 12) != 0u); };
  if (__builtin_expect(qbad(), 0)) {
    u32 sp = 0;
    do { asm volatile("" ::: "memory"); qg[0] = ld_dev(ws, qo); qg[1] = __builtin_amdgcn_raw_buffer_load_b128(ws, qo, 4096, 16); } while (qbad() && ++sp < SPIN_CAP);
  }
  { float v[4];
#pragma unroll
    for (int e = 0; e < 2; ++e) {
      v[2 * e] = asf(qg[0][2 * e] << 10) + asf(qg[1][2 * e] << 10);
      v[2 * e + 1] = asf(__builtin_amdgcn_alignbit(qg[0][2 * e + 1], qg[0][2 * e], 12) & 0xfffffc00u) + asf(__builtin_amdgcn_alignbit(qg[1][2 * e + 1], qg[1][2 * e], 12) & 0xfffffc00u);
    }
    const u32 qq = pk2(fold32(v[0], v[1]), fold32(v[2], v[3]));
    LAS unsigned short* qp = (LAS unsigned short*)(lds + L_Q + sw * 256 + (g & 1) * 128 + (g >> 1) * 32 + c * 2);
    qp[0] = (unsigned short)qq; qp[32] = (unsigned short)(qq >> 16);
    asm volatile("s_waitcnt lgkmcnt(0)" ::: "memory");
    *(LAS u32*)(lds + L_FQ + sw * 4) = 1u; }
  SCHED();
  asm volatile("s_waitcnt vmcnt(0)" ::: "memory");
  STAMP(15);
  v4u kf[4][4];
#pragma unroll
  for (int t = 0; t < 4; ++t)
#pragma unroll
    for (int kk = 0; kk < 4; ++kk) kf[t][kk] = *(LAS v4u*)(stg + (4 * t + (c & 3)) * 1024 + (c >> 2) * 256 + (((4 * kk + g) ^ c) * 16));
  STAMP(2);
  auto wait_flags = [&](int off) {
    u32 sp = 0; v4u f; const u32 fa = (u32)(uintptr_t)(lds + L_FQ + off);
#pragma unroll 1
    do { asm volatile("ds_read_b128 %0, %1\n\ts_waitcnt lgkmcnt(0)" : "=v"(f) : "v"(fa) : "memory"); } while (max4(f) == SENT && ++sp < SPIN_CAP); };
  wait_flags(0);
  STAMP(3);
  v4f sc[4] = {Z4, Z4, Z4, Z4};
#pragma unroll
  for (int kk = 0; kk < 4; ++kk) {
    const v4u qf = *(LAS v4u*)(lds + L_Q + (c >> 2) * 256 + (4 * kk + g) * 16);
#pragma unroll
    for (int t = 0; t < 4; ++t) sc[t] = mfma(kf[t][kk], qf, sc[t]);
  }
  { float p[4];
    const bool u1 = c & 1, u2 = c & 2;
#pragma unroll
    for (int r = 0; r < 4; ++r) { const float lo = u1 ? sc[1][r] : sc[0][r], hi = u1 ? sc[3][r] : sc[2][r]; p[r] = u2 ? hi : lo; }
#pragma unroll
    for (int r = 0; r < 4; r += 2) {
      const u32 t = pk2(p[r], p[r + 1]);
      const u32 u = pk2(bflo(t) * 0.08838834764831845f, bfhi(t) * 0.08838834764831845f);
      p[r] = bflo(u); p[r + 1] = bfhi(u);
    }
    float mx = fmx(fmx3(p[0], p[1], p[2]), p[3]);
    mx = fmaxf(mx, dppf<XOR1>(mx)); mx = fmaxf(mx, dppf<XOR2>(mx));
    mx = max32(max16(mx));
#pragma unroll
    for (int r = 0; r < 4; ++r) p[r] = __builtin_amdgcn_exp2f((p[r] - mx) * 1.4426950408889634f);
    float ls = (p[0] + p[1]) + (p[2] + p[3]);
    ls += dppf<XOR1>(ls); ls += dppf<XOR2>(ls);
    ls = fold16(ls, ls); ls = fold32(ls, ls);
    const float rls = __builtin_amdgcn_rcpf(ls);
    *(LAS v2u*)(lds + L_P + sw * 512 + lane * 8) = (v2u){min(pk2(p[0] * rls, p[1] * rls), SENT - 1u), min(pk2(p[2] * rls, p[3] * rls), SENT - 1u)};
    *(LAS u32*)(lds + L_LW + (c >> 2) * 16 + sw * 4) = min(asu(fmaf(mx, 1.4426950408889634f, __builtin_amdgcn_logf(ls))), SENT - 1u); }
  STAMP(13);
  if (sw == 0) {
    v4u l4; u32 sp = 0;
    do { asm volatile("" ::: "memory"); l4 = *(LAS v4u*)(lds + L_LW + (lane & 3) * 16); } while (__any(max4(l4) == SENT) && ++sp < SPIN_CAP);
    const float m = fmx(fmx3(asf(l4.x), asf(l4.y), asf(l4.z)), asf(l4.w));
    const float sm = (__builtin_amdgcn_exp2f(asf(l4.x) - m) + __builtin_amdgcn_exp2f(asf(l4.y) - m)) + (__builtin_amdgcn_exp2f(asf(l4.z) - m) + __builtin_amdgcn_exp2f(asf(l4.w) - m));
    const u32 lb = min(asu(m + __builtin_amdgcn_logf(sm)), SENT - 1u);
    st_dev(ws, lane == 0 ? box + 1024 : OOB, (v4u){lb, dppu<0x55>(lb), dppu<0xAA>(lb), dppu<0xFF>(lb)});
  }
  STAMP(4);
  __builtin_amdgcn_s_setprio(0);
  LAS char* redw = stg + lane * 4;
  LAS char* wts = lds + L_WTS + sw * 16384;

  if (role == 0) {
    const int rb = i >> 3;
    const u32 gb = WS_OB + (kvh * 2 + rb) * GRP + c * BOX;
    u32 lw, oaddr = lane < 32 ? rline + lane * 4 : OOB, ab = gb + sw * 256 + g * 16; LAS char* redr = lds + L_STG + (lane & 31) * 8;
    { const rsrc_t r = mkrsrc(a.wo, 4u << 20);
      u32 vo = (lane >> 3) * 1024 + ((lane & 7) ^ ((lane >> 3) & 2)) * 16, vo1 = vo ^ 64;
      const u32 so = (kvh * 512 + sw * 128 + (rb ^ 1) * 64) * 1024 + cgq * 128;
      PIN(oaddr); PIN(redr); PIN(redw); PIN(ab); PIN(vo); PIN(vo1);
      wait_flags(W2AT);
      STAMP(14);
      lw = __builtin_amdgcn_raw_buffer_load_b32(ws, gb + 1024 + sw * 4, 0, 16);
#pragma unroll
      for (int k = 0; k < 8; ++k) dma16<WAUX>(r, wts + (rb ^ 1) * 8192 + k * 1024, (k & 1) ? vo1 : vo, so + k * 8192); }
    SCHED();
    STAMP(5);
    v4u af[4];
    request<4, 64>(ws, ab, af);
    SCHED();
    if (__builtin_expect(__any(lw == SENT), 0)) {
      u32 sp = 0;
      do { asm volatile("" ::: "memory"); lw = __builtin_amdgcn_raw_buffer_load_b32(ws, gb + 1024 + sw * 4, 0, 16); } while (lw == SENT && ++sp < SPIN_CAP);
    }
    v4f wt;
    { float m = asf(lw);
      m = fmaxf(m, dppf<XOR1>(m)); m = fmaxf(m, dppf<XOR2>(m)); m = fmaxf(m, dppf<HMIR>(m)); m = fmaxf(m, dppf<ROR8>(m));
      const float e = __builtin_amdgcn_exp2f(asf(lw) - m);
      *(LAS float*)(redw + 2048) = e * __builtin_amdgcn_rcpf(row_sum(e));
      wt = *(LAS v4f*)(stg + 2048 + g * 16); }
    PIN(wt);
    wait_flags(32);
    asm volatile("s_waitcnt vmcnt(4)" ::: "memory");
    STAMP(7);
    v4u bf[4][4];
#pragma unroll
    for (int nt = 0; nt < 4; ++nt) {
      LAS char* p = wts + (g * 8 + (c >> 2)) * 128 + ((nt ^ ((c >> 3) + 2 * (g & 1))) * 32) + (c & 3) * 8;
#pragma unroll
      for (int ks = 0; ks < 4; ++ks) bf[ks][nt] = bfrag_at(p + ks * 4096, 512);
    }
    SCHED();
    STAMP(8);
    if (incomplete<4>(af)) gather<4, 64>(ws, ab, af);
    STAMP(9);
    float val[4];
#pragma unroll
    for (int nt = 0; nt < 4; ++nt) {
      v4f d = Z4;
#pragma unroll
      for (int ks = 0; ks < 4; ++ks) d = mfma(af[ks], bf[ks][nt], d);
      const v2f t = d.xy * wt.xy + d.zw * wt.zw; val[nt] = t.x + t.y;
    }
    *(LAS float*)redw = fold16(fold32(val[0], val[2]), fold32(val[1], val[3]));
    BARRIER();
    if (sw == 0) {
      const v2f o2 = (*(LAS v2f*)redr + *(LAS v2f*)(redr + 16384)) + (*(LAS v2f*)(redr + 32768) + *(LAS v2f*)(redr + 49152));
      st_dev32(ws, oaddr, min(pk2(o2.x, o2.y), SENT - 1u));
    }
    STAMP(10);
    STAMP_OUT();
    return;
  }

  v4u xr[4];
  if (role < 3) {
#pragma unroll
    for (int ks = 0; ks < 4; ++ks) xr[ks] = *(const v4u*)(a.x + (role == 1 ? (c >> 3) * 1024 + sw * 256 + ks * 64 + g * 16 : (sw & 1) * 1024 + lane * 16));
    const u32 r0 = WS_QG + (lane & 7) * (2 * GRP) + (role == 1 ? ((lane >> 3) & 1) * 2048 + (sw * 2 + (g & 1)) * 256 : (sw & 1) * 2048 + (lane >> 3) * 256);
    const u32 r1 = WS_QG + (sw * 2 + (g & 1)) * (2 * GRP) + 4096 + min(lane & 7, 6) * 256;
    u32 bad, sp = 0;
    do {
      asm volatile("" ::: "memory");
      const v4u u0 = ld_dev(ws, r0), u1 = ld_dev(ws, role == 1 ? r0 : r1);
      bad = ((u0.y ^ ta) | (u0.w ^ tb) | (u1.y ^ ta) | (u1.w ^ tb)) >> 12;
    } while (bad != 0u && ++sp < SPIN_CAP);
  }
  const u32 wvo = role == 1 ? (lane >> 2) * 3584 + ((lane & 3) ^ ((lane >> 4) & 2)) * 16 : ((lane >> 1) ^ ((lane >> 2) & 4)) * 1024 + (lane & 1) * 16;
  const u32 wso = role == 1 ? sw * 128 * 3584 + gidx * 64 : sw * 14 * 32768 + didx * 32;
  const char* wp0 = (TG != 1 && (gidx & 1)) ? a.wu : a.wg;
  const char* wp1 = (TG != 1 && (gidx & 1)) ? a.wg : a.wu;
  if (TG >= 1 && role == 1) {
    const rsrc_t r0 = mkrsrc(wp0, 512 * 3584);
#pragma unroll
    for (int j = 0; j < 8; ++j) { dma16<WAUX>(r0, wts + j * 1024, wvo, wso + j * 57344); if (WWIN && j >= WWIN / 2) asm volatile("s_waitcnt vmcnt(%0)" :: "i"(WWIN / 2) : "memory"); }
  }
  wait_flags(FFN_AT < 2 ? 16 : 48);
  if (FFN_DELAY) __builtin_amdgcn_s_sleep(FFN_DELAY);
  u32 tch[3] = {0u, 0u, 0u};
  { const u32 col = ((kvh * 448) & ~127) + (lane & 3) * 128;
    if (TOUCH == 1 && (TG == 0 || (TG == 1 && sw >= 2))) tch[0] = touch((sw < 2 ? a.wg : a.wu) + (size_t)((i - 16) * 32 + (sw & 1) * 16 + (lane >> 2)) * 3584 + col);
    if (TOUCH == 2 && role != 1) {
      const int u = (i - 23) * 4 + sw;
      const int p0 = min(u, 31);
      const u32 ro = (u32)(((p0 & 3) * 128 + (7 - (p0 >> 2)) * 16 + (lane >> 2)) * 3584) + col;
      tch[0] = touch(a.wg + ro); tch[1] = touch(a.wu + ro);
    }
    SCHED();
    if (DN_DELAY && role != 1) __builtin_amdgcn_s_sleep(DN_DELAY);
    if (TOUCHD == 1 && !TD) tch[2] = touch(a.wd + (size_t)((i - 16) * 112 + sw * 28 + min(lane, 27)) * 1024 + kvh * 128);
    if (TOUCHD == 2 && role == 3) { const int u = (i - 27) * 4 + sw;
      tch[2] = touch(a.wd + (size_t)min(u * 90 + lane, 1791) * 1024 + kvh * 128) ^ touch(a.wd + (size_t)min(u * 90 + 64 + (lane & 31), 1791) * 1024 + kvh * 128); }
    SCHED(); }
  if (FFN_AT < 2) wait_flags(48);
  if (role == 3) { asm volatile("" :: "v"(tch[0]), "v"(tch[1]), "v"(tch[2])); STAMP(5); STAMP_OUT(); return; }

  if (role == 1) {
    { const rsrc_t r0 = mkrsrc(wp0, 512 * 3584), r1 = mkrsrc(wp1, 512 * 3584);
      if (TG < 1)
#pragma unroll
        for (int j = 0; j < 8; ++j) dma16<WAUX>(r0, wts + j * 1024, wvo, wso + j * 57344);
#pragma unroll
        for (int j = 0; j < 8; ++j) dma16<WAUX>(r1, wts + 8192 + j * 1024, wvo, wso + j * 57344); }
    v4u af[4];
    const u32 ab = WS_HB + ((c & 7) * 2 + (c >> 3)) * 1024 + sw * 256 + g * 16;
    STAMP(5);
    asm volatile("s_waitcnt vmcnt(0)" ::: "memory");
    STAMP(6);
    request<4, 64>(ws, ab, af);
    SCHED();
    asm volatile("" :: "v"(tch[0]), "v"(tch[1]), "v"(tch[2]));
    v4u xa[4];
#pragma unroll
    for (int ks = 0; ks < 4; ++ks) xa[ks] = (c & 7) ? (v4u){0u, 0u, 0u, 0u} : xr[ks];
    const v4u ones = {0x3f803f80u, 0x3f803f80u, 0x3f803f80u, 0x3f803f80u};
    v4u bf[4][4];
    v4f ac[5] = {Z4, Z4, Z4, Z4, Z4}, cs[4] = {Z4, Z4, Z4, Z4};
#pragma unroll
    for (int ks = 0; ks < 4; ++ks) ac[4] = mfma(xa[ks], ones, ac[4]);
#pragma unroll
    for (int mt = 0; mt < 2; ++mt) {
#pragma unroll
      for (int nt = 0; nt < 2; ++nt) {
        LAS char* p = wts + mt * 8192 + (g * 8 + (c >> 2)) * 64 + ((nt ^ (g & 1)) * 32) + (c & 3) * 8;
#pragma unroll
        for (int ks = 0; ks < 4; ++ks) {
          const int q = 2 * mt + nt;
          bf[ks][q] = bfrag_at(p + ks * 2048, 256);
          ac[q] = mfma(xa[ks], bf[ks][q], ac[q]); cs[q] = mfma(ones, bf[ks][q], cs[q]);
        }
      }
    }
    STAMP(7);
    *(LAS v2f*)(stg + 1024 + lane * 8) = (g & 1) ? (v2f){cs[1].x, cs[3].x} : (v2f){cs[0].x, cs[2].x};
    u32 oaddr = (lane & 1) == 0 ? rline + lane * 2 : OOB; LAS char* rw = stg + lane * 16; LAS char* rr = lds + L_STG + lane * 16; LAS char* cr = lds + L_STG + 1024 + lane * 8;
    PIN(oaddr); PIN(rw); PIN(rr); PIN(cr);
#pragma unroll
    for (int q = 0; q < 5; ++q) PIN(ac[q]);
    STAMP(8);
    if (incomplete<4>(af)) gather<4, 64>(ws, ab, af);
    STAMP(9);
    float r5[5];
#pragma unroll
    for (int q = 0; q < 5; ++q) {
      v4f d = ac[q];
#pragma unroll
      for (int ks = 0; ks < 4; ++ks) d = mfma(af[ks], q < 4 ? bf[ks][q] : ones, d);
      r5[q] = hsum(d);
    }
    *(LAS v4f*)rw = (v4f){fold16(r5[0], r5[1]), fold16(r5[2], r5[3]), fold16(r5[4], r5[4]), 0.f};
    BARRIER();
    if (sw == 0) {
      const v4f z = (*(LAS v4f*)rr + *(LAS v4f*)(rr + 16384)) + (*(LAS v4f*)(rr + 32768) + *(LAS v4f*)(rr + 49152));
      const v2f csum = (*(LAS v2f*)cr + *(LAS v2f*)(cr + 16384)) + (*(LAS v2f*)(cr + 32768) + *(LAS v2f*)(cr + 49152));
      const float mb = bfr(z.z * (1.f / 512.f));
      const float hid = fmaf(-mb, csum.x, z.x) * fmaf(-mb, csum.y, z.y);
      st_dev32(ws, oaddr, min(pk2(hid, dppf<XOR1>(hid)), SENT - 1u));
    }
    STAMP(10);
    STAMP_OUT();
    return;
  }

  {
    {
      const rsrc_t r = mkrsrc(a.wd, 1792 * 1024);
      int j = ((i - 23) * 7) >> 1;
#pragma unroll
      for (int k = 0; k < 14; ++k) { dma16<WAUX>(r, wts + j * 1024, wvo, wso + j * 32768); j = j == 13 ? 0 : j + 1; } }
    STAMP(5);
    asm volatile("s_waitcnt vmcnt(0)" ::: "memory");
    asm volatile("" :: "v"(tch[0]), "v"(tch[1]), "v"(tch[2]));
    STAMP(7);
    v4u bf[14];
    { LAS char* p0 = wts + g * 256 + (g & 1) * 128 + c * 8; LAS char* p1 = wts + g * 256 + ((g & 1) ^ 1) * 128 + c * 8;
#pragma unroll
      for (int kb = 0; kb < 14; ++kb) { const v2u lo = tr_read(p0 + kb * 1024), hi = tr_read(p1 + kb * 1024); bf[kb] = (v4u){lo.x, lo.y, hi.x, hi.y}; } }
    if (sw < 2) {
      v4u hp[8];
      gather<8, 2048>(ws, WS_HB + sw * 1024 + lane * 16, hp);
      v2f h[4], s2 = {0.f, 0.f};
#pragma unroll
      for (int d = 0; d < 4; ++d) {
        v2f z = {bflo(hp[0][d]), bfhi(hp[0][d])};
#pragma unroll
        for (int u = 1; u < 8; ++u) z += (v2f){bflo(hp[u][d]), bfhi(hp[u][d])};
        const u32 t = pk2(z.x, z.y), hh = pk2(bflo(xr[0][d]) + bflo(t), bfhi(xr[0][d]) + bfhi(t));
        h[d] = (v2f){bflo(hh), bfhi(hh)};
        s2 += h[d];
      }
      const float mean = wave_sum(s2.x + s2.y) * (1.f / 512.f);
      v2f q2 = {0.f, 0.f};
#pragma unroll
      for (int d = 0; d < 4; ++d) { const v2f t = h[d] - mean; q2 = t * t + q2; }
      const float inv = __builtin_amdgcn_rcpf(ln_sd(wave_sum(q2.x + q2.y) * (1.f / 512.f)));
      if ((lane >> 1) == didx) {
        *(LAS v4f*)(lds + L_H + sw * 64 + (lane & 1) * 32) = (v4f){h[0].x, h[0].y, h[1].x, h[1].y};
        *(LAS v4f*)(lds + L_H + sw * 64 + (lane & 1) * 32 + 16) = (v4f){h[2].x, h[2].y, h[3].x, h[3].y};
        *(LAS float*)(lds + L_H + 128 + sw * 4) = inv * inv;
      }
    }
    const rsrc_t ro = mkrsrc(a.out, 2048);
    u32 oaddr = lane < 16 ? (lane >> 3) * 1024 + didx * 32 + (lane & 7) * 4 : OOB;
    LAS char* rr = lds + L_STG + ((lane >> 3) & 1) * 128 + (lane & 7) * 8; LAS char* hr = lds + L_H + (lane & 15) * 8; LAS char* sr = lds + L_H + 128 + ((lane >> 3) & 1) * 4;
    u32 gb = (c & 7) < 7 ? WS_HID + (sw * 14 + (c & 7)) * 128 + (c >> 3) * 64 + g * 16 : OOB;
    PIN(oaddr); PIN(rr); PIN(hr); PIN(sr); PIN(redw); PIN(gb);
    STAMP(8);
    v4u af[2];
    gather<2, 896>(ws, gb, af);
    STAMP(9);
    v4f d[7];
#pragma unroll
    for (int kb = 0; kb < 7; ++kb) d[kb] = mfma(af[1], bf[7 + kb], mfma(af[0], bf[kb], Z4));
    const bool odd = g & 1;
    const float y = ((odd ? d[4].x : d[0].x) + (odd ? d[5].y : d[1].y)) + ((odd ? d[6].z : d[2].z) + (odd ? 0.f : d[3].w));
    *(LAS float*)redw = fold16(y, y);
    BARRIER();
    if (sw == 0) {
      const v2f z = ((*(LAS v2f*)rr + *(LAS v2f*)(rr + 16384)) + (*(LAS v2f*)(rr + 32768) + *(LAS v2f*)(rr + 49152))) * *(LAS float*)sr;
      const v2f hh = *(LAS v2f*)hr;
      const u32 f2 = pk2(z.x, z.y);
      st_dev32(ro, oaddr, pk2(hh.x + bflo(f2), hh.y + bfhi(f2)));
      st_dev64(ws, (w == 23 * 8 && lane == 0) ? WS_EPOCH : OOB, (v2u){(u32)tag, (u32)(tag >> 32)});
    }
    STAMP(10);
    STAMP_OUT();
  }
}

extern "C" void kernel_launch(void* const* d_in, const int* in_sizes, int n_in, void* d_out, int out_size, void* d_ws, size_t ws_size,
                              hipStream_t stream) {
  Args a;
  a.x = (const char*)d_in[0]; a.kc = (const char*)d_in[1]; a.vc = (const char*)d_in[2]; a.wq = (const char*)d_in[3]; a.wo = (const char*)d_in[4];
  a.wg = (const char*)d_in[6]; a.wu = (const char*)d_in[7]; a.wd = (const char*)d_in[8];
  a.out = (char*)d_out; a.ws = (char*)d_ws;
  fused_decode<<<256, 512, 0, stream>>>(a);
}
```
